```python
import torch

def _softmax(x, axis=-1):
    return torch.softmax(x, dim=axis)
def _gelu(x):
    return torch.nn.functional.gelu(x, approximate="tanh")
def _silu(x):
    return torch.nn.functional.silu(x)
def _sigmoid(x):
    return torch.sigmoid(x)
def _layernorm(x, axis=-1, eps=1e-5):
    m = x.mean(dim=axis, keepdim=True)
    v = x.var(dim=axis, keepdim=True, unbiased=False)
    return (x - m) / torch.sqrt(v + eps)
def _scatter_add(tgt, idx, src, axis=0):
    return tgt.clone().index_add_(axis, idx, src)
def _segment_sum(src, seg, n):
    out = torch.zeros((n,) + src.shape[1:], dtype=src.dtype)
    return out.index_add_(0, seg, src)
def _pad(x, pw):
    flat = tuple(v for pair in reversed(pw) for v in pair)
    return torch.nn.functional.pad(x, flat)

kernel_name = 'interleave_layers__gqa_block_cached__glu_gated_reglu__decode'

def setup_inputs():

    _w = (lambda a: (lambda t: t.to(torch.bfloat16) if t.is_floating_point() else t)(torch.as_tensor(a)))
    def _A_setup():
        B, Sq, D = 1, 1, 576
        Hq, Hkv, Sk, Dh = 32, 16, 65536, 128
        assert Hq % Hkv == 0, (Hq, Hkv)
        return {
            "x": _w((lambda *s: torch.randn(*s, dtype=torch.bfloat16) * 0.05)(B, Sq, D)),
            "k_cache": _w((lambda *s: torch.randn(*s, dtype=torch.bfloat16) * 0.8)(B, Hkv, Sk, Dh)),
            "v_cache": _w((lambda *s: torch.randn(*s, dtype=torch.bfloat16) * 1.6)(B, Hkv, Sk, Dh)),
            "wq": _w((lambda *s: torch.randn(*s, dtype=torch.bfloat16) * 0.8)(D, Hq * Dh) * (D ** -0.5)),
            "wo": _w((lambda *s: torch.randn(*s, dtype=torch.bfloat16) * 1.6)(Hq * Dh, D) * ((Hq * Dh) ** -0.5)),
        }
    _A_in = _A_setup()

    def _B_setup():
        B, S, D, F = 1, 1, 576, 4608
        return {
            "x": _w((lambda *s: torch.randn(*s, dtype=torch.bfloat16) * 0.05)(B, S, D)),
            "W_gate": _w((lambda *s: torch.randn(*s, dtype=torch.bfloat16) * 0.4)(D, F) * (2.0 / D) ** 0.5),
            "W_up": _w((lambda *s: torch.randn(*s, dtype=torch.bfloat16) * 0.4)(D, F) * (2.0 / D) ** 0.5),
            "W_down": _w((lambda *s: torch.randn(*s, dtype=torch.bfloat16) * 0.4)(F, D) * (1.0 / F) ** 0.5),
        }
    _B_in = _B_setup()

    return {
        "A_x": _A_in["x"],
        "A_k_cache": _A_in["k_cache"],
        "A_v_cache": _A_in["v_cache"],
        "A_wq": _A_in["wq"],
        "A_wo": _A_in["wo"],
        "B_x": _B_in["x"],
        "B_W_gate": _B_in["W_gate"],
        "B_W_up": _B_in["W_up"],
        "B_W_down": _B_in["W_down"],
    }

def reference(A_x, A_k_cache, A_v_cache, A_wq, A_wo, B_x, B_W_gate, B_W_up, B_W_down):
    def _A_ref(x, k_cache, v_cache, wq, wo):
        B, Sq, D = x.shape
        Hq, Dh = 32, 128
        Hkv = k_cache.shape[1]
        rep = Hq // Hkv
        q = (lambda x, *d: x.permute(*d))((x @ wq).reshape(B, Sq, Hq, Dh), 0, 2, 1, 3)
        k = (lambda x, n, axis: torch.repeat_interleave(x, n, dim=axis))(k_cache, rep, axis=1)
        v = (lambda x, n, axis: torch.repeat_interleave(x, n, dim=axis))(v_cache, rep, axis=1)
        scores = torch.einsum("bhqd,bhkd->bhqk", q, k) * (Dh ** -0.5)
        p = _softmax(scores, axis=-1)
        o = torch.einsum("bhqk,bhkd->bhqd", p, v)
        return (lambda x, *d: x.permute(*d))(o, 0, 2, 1, 3).reshape(B, Sq, Hq * Dh) @ wo

    def _B_ref(x, W_gate, W_up, W_down):
        gate = torch.einsum("bsd,df->bsf", x, W_gate)
        up = torch.einsum("bsd,df->bsf", x, W_up)
        gate = gate * (lambda x, dt: x.to(dt))(gate > 0, gate.dtype)
        h = gate * up
        return torch.einsum("bsf,fd->bsd", h, W_down)

    x = A_x
    x = x + _A_ref(_layernorm(x, axis=-1), A_k_cache, A_v_cache, A_wq, A_wo)
    x = x + _B_ref(_layernorm(x, axis=-1), B_W_gate, B_W_up, B_W_down)
    x = x + _A_ref(_layernorm(x, axis=-1), A_k_cache, A_v_cache, A_wq, A_wo)
    x = x + _B_ref(_layernorm(x, axis=-1), B_W_gate, B_W_up, B_W_down)
    return x
```

```cpp
#include <hip/hip_runtime.h>
#include <hip/hip_bf16.h>
#include <cstdint>

#define KV_AUX 2

typedef unsigned v4u __attribute__((ext_vector_type(4)));
typedef float f32x4 __attribute__((ext_vector_type(4)));
typedef short bf16x8 __attribute__((ext_vector_type(8)));
typedef unsigned short u16;

constexpr int D = 576, HQ = 32, HKV = 16, SK = 65536, DH = 128, F = 4608, QD = HQ * DH;
constexpr int NSPLIT = 16;
constexpr int KEYS_WG = SK / NSPLIT;

__device__ __forceinline__ float bf2f(u16 b) { return __builtin_bit_cast(float, (unsigned)b << 16); }
__device__ __forceinline__ float rbf(float f) { return (float)(__bf16)f; }
__device__ __forceinline__ u16 f2bf(float f) { return __builtin_bit_cast(u16, (__bf16)f); }
__device__ __forceinline__ float lo16(unsigned u) { return __builtin_bit_cast(float, u << 16); }
__device__ __forceinline__ float hi16(unsigned u) { return __builtin_bit_cast(float, u & 0xffff0000u); }
__device__ __forceinline__ f32x4 mfma16(v4u a, v4u b, f32x4 c) {
  return __builtin_amdgcn_mfma_f32_16x16x32_bf16(__builtin_bit_cast(bf16x8, a), __builtin_bit_cast(bf16x8, b), c, 0, 0, 0);
}

__device__ void layernorm_block(const float* xs, float* ln, float* red) {
  const int tid = threadIdx.x;
  float s = 0.f;
  for (int i = tid; i < D; i += 256) s += xs[i];
  for (int o = 32; o > 0; o >>= 1) s += __shfl_xor(s, o);
  if ((tid & 63) == 0) red[tid >> 6] = s;
  __syncthreads();
  const float mean = (red[0] + red[1] + red[2] + red[3]) * (1.f / D);
  __syncthreads();
  float v = 0.f;
  for (int i = tid; i < D; i += 256) { const float d = xs[i] - mean; v += d * d; }
  for (int o = 32; o > 0; o >>= 1) v += __shfl_xor(v, o);
  if ((tid & 63) == 0) red[tid >> 6] = v;
  __syncthreads();
  const float var = rbf((red[0] + red[1] + red[2] + red[3]) * (1.f / D));
  const float mb = rbf(mean);
  const float den = rbf(sqrtf(rbf(var + 1e-5f)));
  for (int i = tid; i < D; i += 256) ln[i] = rbf(rbf(xs[i] - mb) / den);
  __syncthreads();
}

__device__ void gather_x(const u16* xin, const float* xprev, const float* part, int np, float* xs) {
  for (int i = threadIdx.x; i < D; i += 256) {
    float x = xin ? bf2f(xin[i]) : xprev[i];
    if (np > 0) { float s = 0.f; for (int p = 0; p < np; ++p) s += part[(size_t)p * D + i]; x = rbf(x + rbf(s)); }
    xs[i] = x;
  }
  __syncthreads();
}

__global__ void __launch_bounds__(256) k_q(const u16* xin, const float* xprev, const float* part, int np, float* xout, const u16* wq, u16* q) {
  __shared__ float xs[D], ln[D], red[8], acc[32][65];
  gather_x(xin, xprev, part, np, xs);
  if (blockIdx.x == 0) for (int i = threadIdx.x; i < D; i += 256) xout[i] = xs[i];
  layernorm_block(xs, ln, red);
  const int r = threadIdx.x >> 3, c = threadIdx.x & 7, col0 = blockIdx.x * 64 + c * 8;
  float a[8] = {};
  for (int i = r; i < D; i += 32) {
    const v4u w = *(const v4u*)(wq + (size_t)i * QD + col0); const float l = ln[i];
#pragma unroll
    for (int k = 0; k < 4; ++k) { a[2 * k] += l * lo16(w[k]); a[2 * k + 1] += l * hi16(w[k]); }
  }
#pragma unroll
  for (int k = 0; k < 8; ++k) acc[r][c * 8 + k] = a[k];
  __syncthreads();
  if (threadIdx.x < 64) { float s = 0.f; for (int j = 0; j < 32; ++j) s += acc[j][threadIdx.x]; q[blockIdx.x * 64 + threadIdx.x] = f2bf(s); }
}

constexpr int AW = 8;
__global__ void __launch_bounds__(AW * 64, 1) k_attn(const u16* q, const u16* K, const u16* V, float* pml, float* po) {
  __shared__ float sm[AW][2], sl[AW][2], so[AW][256];
  const int wg = blockIdx.x, g = wg >> 4;
  const int tid = threadIdx.x, lane = tid & 63, wave = __builtin_amdgcn_readfirstlane(tid >> 6);
  const int n = lane & 15, gq = lane >> 4, h = n >> 3;
  __amdgpu_buffer_rsrc_t rk = __builtin_amdgcn_make_buffer_rsrc((void*)K, 0, 1 << 28, 0x00020000);
  __amdgpu_buffer_rsrc_t rv = __builtin_amdgcn_make_buffer_rsrc((void*)V, 0, 1 << 28, 0x00020000);
  v4u qf[4];
#pragma unroll
  for (int i = 0; i < 4; ++i) qf[i] = *(const v4u*)(q + (2 * g + h) * DH + 32 * i + 8 * gq);
  unsigned msk[4];
#pragma unroll
  for (int i = 0; i < 4; ++i) msk[i] = (((lane & 7) >> 1) == i) ? ((lane & 1) ? 0xffff0000u : 0x0000ffffu) : 0u;
  const int rowkey = 4 * (n & 3) + (n >> 2);
  const int voffk = rowkey * 256 + gq * 16, voffv = lane * 16;
  constexpr int TB = 8192;
  constexpr int NT = KEYS_WG * 256 / (AW * TB);
  const int base = wg * (KEYS_WG * 256) + wave * TB;
  constexpr float C2 = 0.088388347648318440f * 1.4426950408889634f;
  float m = -INFINITY, l = 0.f; f32x4 acc = {0, 0, 0, 0};
  v4u bk[2][8], bv[2][8];
  auto issue = [&](int t, v4u* dk, v4u* dv) {
    const int b = base + t * (AW * TB);
#pragma unroll
    for (int i = 0; i < 8; ++i) dk[i] = __builtin_amdgcn_raw_buffer_load_b128(rk, voffk + (i >> 2) * 4096 + (i & 3) * 64, b, KV_AUX);
#pragma unroll
    for (int i = 0; i < 8; ++i) dv[i] = __builtin_amdgcn_raw_buffer_load_b128(rv, voffv + i * 1024, b, KV_AUX);
  };
  auto compute = [&](v4u* dk, v4u* dv) {
    f32x4 s0 = {0, 0, 0, 0}, s1 = {0, 0, 0, 0};
#pragma unroll
    for (int i = 0; i < 4; ++i) { s0 = mfma16(dk[i], qf[i], s0); s1 = mfma16(dk[4 + i], qf[i], s1); }
    float mx = fmaxf(fmaxf(fmaxf(s0[0], s0[1]), fmaxf(s0[2], s0[3])), fmaxf(fmaxf(s1[0], s1[1]), fmaxf(s1[2], s1[3])));
    mx = fmaxf(mx, __shfl_xor(mx, 16)); mx = fmaxf(mx, __shfl_xor(mx, 32));
    const float mn = fmaxf(m, mx * C2);
    const float alpha = __builtin_amdgcn_exp2f(m - mn);
    m = mn; l *= alpha; acc *= alpha;
#pragma unroll
    for (int t = 0; t < 8; ++t) {
      const float p = __builtin_amdgcn_exp2f((t < 4 ? s0[t] : s1[t - 4]) * C2 - mn);
      const unsigned pb = f2bf(p); const unsigned pp = pb | (pb << 16);
      l += bf2f((u16)pb);
      const v4u b = {pp & msk[0], pp & msk[1], pp & msk[2], pp & msk[3]};
      acc = mfma16(dv[t], b, acc);
    }
  };
  issue(0, bk[0], bv[0]);
  for (int t = 0; t < NT; t += 2) {
    issue(t + 1, bk[1], bv[1]);
    __builtin_amdgcn_sched_barrier(0);
    compute(bk[0], bv[0]);
    __builtin_amdgcn_sched_barrier(0);
    if (t + 2 < NT) issue(t + 2, bk[0], bv[0]);
    __builtin_amdgcn_sched_barrier(0);
    compute(bk[1], bv[1]);
    __builtin_amdgcn_sched_barrier(0);
  }
  l += __shfl_xor(l, 16); l += __shfl_xor(l, 32);
  if (lane == 0 || lane == 8) { sm[wave][h] = m; sl[wave][h] = l; }
#pragma unroll
  for (int r = 0; r < 4; ++r) so[wave][h * 128 + 8 * (4 * gq + r) + (n & 7)] = acc[r];
  __syncthreads();
  if (tid < 256) {
    const int hh = tid >> 7;
    float M = -INFINITY;
    for (int w = 0; w < AW; ++w) M = fmaxf(M, sm[w][hh]);
    float L = 0.f, o = 0.f;
    for (int w = 0; w < AW; ++w) { const float e = __builtin_amdgcn_exp2f(sm[w][hh] - M); L += e * sl[w][hh]; o += e * so[w][tid]; }
    po[wg * 256 + tid] = o;
    if ((tid & 127) == 0) { pml[(wg * 2 + hh) * 2] = M; pml[(wg * 2 + hh) * 2 + 1] = L; }
  }
}

__global__ void __launch_bounds__(256) k_oproj(const float* pml, const float* po, const u16* wo, float* part) {
  __shared__ float o[64], acc[3][D];
  const int j = blockIdx.x, tid = threadIdx.x;
  if (tid < 64) {
    const int row = j * 64 + tid, hq = row >> 7, d = row & 127, g = hq >> 1, hh = hq & 1;
    float M = -INFINITY;
    for (int s = 0; s < NSPLIT; ++s) M = fmaxf(M, pml[((g * 16 + s) * 2 + hh) * 2]);
    float L = 0.f, a = 0.f;
    for (int s = 0; s < NSPLIT; ++s) { const float e = __builtin_amdgcn_exp2f(pml[((g * 16 + s) * 2 + hh) * 2] - M); L += e * pml[((g * 16 + s) * 2 + hh) * 2 + 1]; a += e * po[(g * 16 + s) * 256 + hh * 128 + d]; }
    o[tid] = rbf(a / L);
  }
  __syncthreads();
  const int c = tid % 72, r = tid / 72;
  if (r < 3) {
    float a[8] = {};
    for (int i = r; i < 64; i += 3) {
      const v4u w = *(const v4u*)(wo + (size_t)(j * 64 + i) * D + c * 8); const float x = o[i];
#pragma unroll
      for (int k = 0; k < 4; ++k) { a[2 * k] += x * lo16(w[k]); a[2 * k + 1] += x * hi16(w[k]); }
    }
#pragma unroll
    for (int k = 0; k < 8; ++k) acc[r][c * 8 + k] = a[k];
  }
  __syncthreads();
  for (int i = tid; i < D; i += 256) part[(size_t)j * D + i] = acc[0][i] + acc[1][i] + acc[2][i];
}

__global__ void __launch_bounds__(256) k_mlp(const float* xprev, const float* partin, int np, float* xout, const u16* wg_, const u16* wu, const u16* wd, float* part) {
  __shared__ float xs[D], ln[D], red[8], accg[64][33], accu[64][33], hs[32], acc[3][D];
  const int j = blockIdx.x, tid = threadIdx.x;
  gather_x(nullptr, xprev, partin, np, xs);
  if (j == 0) for (int i = tid; i < D; i += 256) xout[i] = xs[i];
  layernorm_block(xs, ln, red);
  {
    const int r = tid >> 2, c = tid & 3, col0 = j * 32 + c * 8;
    float a[8] = {}, b[8] = {};
    for (int i = r; i < D; i += 64) {
      const v4u w = *(const v4u*)(wg_ + (size_t)i * F + col0), u = *(const v4u*)(wu + (size_t)i * F + col0); const float l = ln[i];
#pragma unroll
      for (int k = 0; k < 4; ++k) { a[2 * k] += l * lo16(w[k]); a[2 * k + 1] += l * hi16(w[k]); b[2 * k] += l * lo16(u[k]); b[2 * k + 1] += l * hi16(u[k]); }
    }
#pragma unroll
    for (int k = 0; k < 8; ++k) { accg[r][c * 8 + k] = a[k]; accu[r][c * 8 + k] = b[k]; }
  }
  __syncthreads();
  if (tid < 32) { float sg = 0.f, su = 0.f; for (int r = 0; r < 64; ++r) { sg += accg[r][tid]; su += accu[r][tid]; }
    sg = rbf(sg); su = rbf(su); const float gt = sg > 0.f ? sg : sg * 0.f; hs[tid] = rbf(gt * su); }
  __syncthreads();
  const int c = tid % 72, r = tid / 72;
  if (r < 3) {
    float a[8] = {};
    for (int i = r; i < 32; i += 3) {
      const v4u w = *(const v4u*)(wd + (size_t)(j * 32 + i) * D + c * 8); const float x = hs[i];
#pragma unroll
      for (int k = 0; k < 4; ++k) { a[2 * k] += x * lo16(w[k]); a[2 * k + 1] += x * hi16(w[k]); }
    }
#pragma unroll
    for (int k = 0; k < 8; ++k) acc[r][c * 8 + k] = a[k];
  }
  __syncthreads();
  for (int i = tid; i < D; i += 256) part[(size_t)j * D + i] = acc[0][i] + acc[1][i] + acc[2][i];
}

__global__ void __launch_bounds__(64) k_final(const float* xprev, const float* part, int np, u16* out) {
  const int i = blockIdx.x * 64 + threadIdx.x;
  float s = 0.f; for (int p = 0; p < np; ++p) s += part[(size_t)p * D + i];
  out[i] = f2bf(xprev[i] + rbf(s));
}

extern "C" void kernel_launch(void* const* d_in, const int* in_sizes, int n_in, void* d_out, int out_size, void* d_ws, size_t ws_size, hipStream_t stream) {
  const u16 *x = (const u16*)d_in[0], *K = (const u16*)d_in[1], *V = (const u16*)d_in[2], *wq = (const u16*)d_in[3], *wo = (const u16*)d_in[4];
  const u16 *wg = (const u16*)d_in[6], *wu = (const u16*)d_in[7], *wd = (const u16*)d_in[8];
  char* ws = (char*)d_ws;
  u16* q = (u16*)ws; float* pml = (float*)(ws + 16384); float* po = (float*)(ws + 32768);
  float* part1 = (float*)(ws + (1 << 20)); float* part2 = (float*)(ws + (2 << 20));
  float *xa = (float*)(ws + (3 << 20)), *xb = xa + 1024, *xc = xa + 2048, *xd = xa + 3072;
  k_q<<<64, 256, 0, stream>>>(x, nullptr, nullptr, 0, xa, wq, q);
  k_attn<<<256, AW * 64, 0, stream>>>(q, K, V, pml, po);
  k_oproj<<<64, 256, 0, stream>>>(pml, po, wo, part1);
  k_mlp<<<144, 256, 0, stream>>>(xa, part1, 64, xb, wg, wu, wd, part2);
  k_q<<<64, 256, 0, stream>>>(nullptr, xb, part2, 144, xc, wq, q);
  k_attn<<<256, AW * 64, 0, stream>>>(q, K, V, pml, po);
  k_oproj<<<64, 256, 0, stream>>>(pml, po, wo, part1);
  k_mlp<<<144, 256, 0, stream>>>(xc, part1, 64, xd, wg, wu, wd, part2);
  k_final<<<9, 64, 0, stream>>>(xd, part2, 144, (u16*)d_out);
}
```

```cpp
#include <hip/hip_runtime.h>
#include <hip/hip_bf16.h>
#include <cstdint>

#define K1 2
#define V1 2
#define K2 2
#define V2 2

typedef unsigned v4u __attribute__((ext_vector_type(4)));
typedef float f32x4 __attribute__((ext_vector_type(4)));
typedef short bf16x8 __attribute__((ext_vector_type(8)));
typedef unsigned short u16;

constexpr int D = 576, HQ = 32, HKV = 16, SK = 65536, DH = 128, F = 4608, QD = HQ * DH;
constexpr int NSPLIT = 16;
constexpr int KEYS_WG = SK / NSPLIT;

__device__ __forceinline__ float bf2f(u16 b) { return __builtin_bit_cast(float, (unsigned)b << 16); }
__device__ __forceinline__ float rbf(float f) { return (float)(__bf16)f; }
__device__ __forceinline__ u16 f2bf(float f) { return __builtin_bit_cast(u16, (__bf16)f); }
__device__ __forceinline__ float lo16(unsigned u) { return __builtin_bit_cast(float, u << 16); }
__device__ __forceinline__ float hi16(unsigned u) { return __builtin_bit_cast(float, u & 0xffff0000u); }
__device__ __forceinline__ f32x4 mfma16(v4u a, v4u b, f32x4 c) {
  return __builtin_amdgcn_mfma_f32_16x16x32_bf16(__builtin_bit_cast(bf16x8, a), __builtin_bit_cast(bf16x8, b), c, 0, 0, 0);
}

__device__ void layernorm_block(const float* xs, float* ln, float* red) {
  const int tid = threadIdx.x;
  float s = 0.f;
  for (int i = tid; i < D; i += 256) s += xs[i];
  for (int o = 32; o > 0; o >>= 1) s += __shfl_xor(s, o);
  if ((tid & 63) == 0) red[tid >> 6] = s;
  __syncthreads();
  const float mean = (red[0] + red[1] + red[2] + red[3]) * (1.f / D);
  __syncthreads();
  float v = 0.f;
  for (int i = tid; i < D; i += 256) { const float d = xs[i] - mean; v += d * d; }
  for (int o = 32; o > 0; o >>= 1) v += __shfl_xor(v, o);
  if ((tid & 63) == 0) red[tid >> 6] = v;
  __syncthreads();
  const float var = rbf((red[0] + red[1] + red[2] + red[3]) * (1.f / D));
  const float mb = rbf(mean);
  const float den = rbf(sqrtf(rbf(var + 1e-5f)));
  for (int i = tid; i < D; i += 256) ln[i] = rbf(rbf(xs[i] - mb) / den);
  __syncthreads();
}

__device__ void gather_x(const u16* xin, const float* xprev, const float* part, int np, float* xs) {
  for (int i = threadIdx.x; i < D; i += 256) {
    float x = xin ? bf2f(xin[i]) : xprev[i];
    if (np > 0) { float s = 0.f;
      for (int p0 = 0; p0 < np; p0 += 16) { float t[16];
#pragma unroll
        for (int u = 0; u < 16; ++u) t[u] = part[(size_t)(p0 + u) * D + i];
#pragma unroll
        for (int u = 0; u < 16; ++u) s += t[u]; }
      x = rbf(x + rbf(s)); }
    xs[i] = x;
  }
  __syncthreads();
}

__global__ void __launch_bounds__(256) k_q(const u16* xin, const float* xprev, const float* part, int np, float* xout, const u16* wq, u16* q) {
  __shared__ float xs[D], ln[D], red[8], acc[32][65];
  gather_x(xin, xprev, part, np, xs);
  if (blockIdx.x == 0) for (int i = threadIdx.x; i < D; i += 256) xout[i] = xs[i];
  layernorm_block(xs, ln, red);
  const int r = threadIdx.x >> 3, c = threadIdx.x & 7, col0 = blockIdx.x * 64 + c * 8;
  float a[8] = {};
#pragma unroll
  for (int ii = 0; ii < 18; ++ii) { const int i = r + 32 * ii;
    const v4u w = *(const v4u*)(wq + (size_t)i * QD + col0); const float l = ln[i];
#pragma unroll
    for (int k = 0; k < 4; ++k) { a[2 * k] += l * lo16(w[k]); a[2 * k + 1] += l * hi16(w[k]); }
  }
#pragma unroll
  for (int k = 0; k < 8; ++k) acc[r][c * 8 + k] = a[k];
  __syncthreads();
  if (threadIdx.x < 64) { float s = 0.f; for (int j = 0; j < 32; ++j) s += acc[j][threadIdx.x]; q[blockIdx.x * 64 + threadIdx.x] = f2bf(s); }
}

constexpr int AW = 8;
template <int KA, int VA>
__global__ void __launch_bounds__(AW * 64, 1) k_attn(const u16* q, const u16* K, const u16* V, float* pml, float* po) {
  __shared__ float sm[AW][2], sl[AW][2], so[AW][256];
  const int wg = blockIdx.x, g = wg >> 4;
  const int tid = threadIdx.x, lane = tid & 63, wave = __builtin_amdgcn_readfirstlane(tid >> 6);
  const int n = lane & 15, gq = lane >> 4, h = n >> 3;
  __amdgpu_buffer_rsrc_t rk = __builtin_amdgcn_make_buffer_rsrc((void*)K, 0, 1 << 28, 0x00020000);
  __amdgpu_buffer_rsrc_t rv = __builtin_amdgcn_make_buffer_rsrc((void*)V, 0, 1 << 28, 0x00020000);
  v4u qf[4];
#pragma unroll
  for (int i = 0; i < 4; ++i) qf[i] = *(const v4u*)(q + (2 * g + h) * DH + 32 * i + 8 * gq);
  unsigned msk[4];
#pragma unroll
  for (int i = 0; i < 4; ++i) msk[i] = (((lane & 7) >> 1) == i) ? ((lane & 1) ? 0xffff0000u : 0x0000ffffu) : 0u;
  const int rowkey = 4 * (n & 3) + (n >> 2);
  const int voffk = rowkey * 256 + gq * 16, voffv = lane * 16;
  constexpr int TB = 8192;
  constexpr int NT = KEYS_WG * 256 / (AW * TB);
  const int base = wg * (KEYS_WG * 256) + wave * TB;
  constexpr float C2 = 0.088388347648318440f * 1.4426950408889634f;
  float m = -INFINITY, l = 0.f; f32x4 acc = {0, 0, 0, 0};
  v4u bk[2][8], bv[2][8];
  auto issue = [&](int t, v4u* dk, v4u* dv) {
    const int b = base + t * (AW * TB);
#pragma unroll
    for (int i = 0; i < 8; ++i) dk[i] = __builtin_amdgcn_raw_buffer_load_b128(rk, voffk + (i >> 2) * 4096 + (i & 3) * 64, b, KA);
#pragma unroll
    for (int i = 0; i < 8; ++i) dv[i] = __builtin_amdgcn_raw_buffer_load_b128(rv, voffv + i * 1024, b, VA);
  };
  auto compute = [&](v4u* dk, v4u* dv) {
    f32x4 s0 = {0, 0, 0, 0}, s1 = {0, 0, 0, 0};
#pragma unroll
    for (int i = 0; i < 4; ++i) { s0 = mfma16(dk[i], qf[i], s0); s1 = mfma16(dk[4 + i], qf[i], s1); }
    float mx = fmaxf(fmaxf(fmaxf(s0[0], s0[1]), fmaxf(s0[2], s0[3])), fmaxf(fmaxf(s1[0], s1[1]), fmaxf(s1[2], s1[3])));
    mx = fmaxf(mx, __shfl_xor(mx, 16)); mx = fmaxf(mx, __shfl_xor(mx, 32));
    const float mn = fmaxf(m, mx * C2);
    const float alpha = __builtin_amdgcn_exp2f(m - mn);
    m = mn; l *= alpha; acc *= alpha;
#pragma unroll
    for (int t = 0; t < 8; ++t) {
      const float p = __builtin_amdgcn_exp2f((t < 4 ? s0[t] : s1[t - 4]) * C2 - mn);
      const unsigned pb = f2bf(p); const unsigned pp = pb | (pb << 16);
      l += bf2f((u16)pb);
      const v4u b = {pp & msk[0], pp & msk[1], pp & msk[2], pp & msk[3]};
      acc = mfma16(dv[t], b, acc);
    }
  };
  issue(0, bk[0], bv[0]);
  for (int t = 0; t < NT; t += 2) {
    issue(t + 1, bk[1], bv[1]);
    __builtin_amdgcn_sched_barrier(0);
    compute(bk[0], bv[0]);
    __builtin_amdgcn_sched_barrier(0);
    if (t + 2 < NT) issue(t + 2, bk[0], bv[0]);
    __builtin_amdgcn_sched_barrier(0);
    compute(bk[1], bv[1]);
    __builtin_amdgcn_sched_barrier(0);
  }
  l += __shfl_xor(l, 16); l += __shfl_xor(l, 32);
  if (lane == 0 || lane == 8) { sm[wave][h] = m; sl[wave][h] = l; }
#pragma unroll
  for (int r = 0; r < 4; ++r) so[wave][h * 128 + 8 * (4 * gq + r) + (n & 7)] = acc[r];
  __syncthreads();
  if (tid < 256) {
    const int hh = tid >> 7;
    float M = -INFINITY;
    for (int w = 0; w < AW; ++w) M = fmaxf(M, sm[w][hh]);
    float L = 0.f, o = 0.f;
    for (int w = 0; w < AW; ++w) { const float e = __builtin_amdgcn_exp2f(sm[w][hh] - M); L += e * sl[w][hh]; o += e * so[w][tid]; }
    po[wg * 256 + tid] = o;
    if ((tid & 127) == 0) { pml[(wg * 2 + hh) * 2] = M; pml[(wg * 2 + hh) * 2 + 1] = L; }
  }
}

__global__ void __launch_bounds__(256) k_oproj(const float* pml, const float* po, const u16* wo, float* part) {
  __shared__ float o[64], acc[3][D];
  const int j = blockIdx.x, tid = threadIdx.x;
  if (tid < 64) {
    const int row = j * 64 + tid, hq = row >> 7, d = row & 127, g = hq >> 1, hh = hq & 1;
    float M = -INFINITY;
#pragma unroll
    for (int s = 0; s < NSPLIT; ++s) M = fmaxf(M, pml[((g * 16 + s) * 2 + hh) * 2]);
    float L = 0.f, a = 0.f;
#pragma unroll
    for (int s = 0; s < NSPLIT; ++s) { const float e = __builtin_amdgcn_exp2f(pml[((g * 16 + s) * 2 + hh) * 2] - M); L += e * pml[((g * 16 + s) * 2 + hh) * 2 + 1]; a += e * po[(g * 16 + s) * 256 + hh * 128 + d]; }
    o[tid] = rbf(a / L);
  }
  __syncthreads();
  const int c = tid % 72, r = tid / 72;
  if (r < 3) {
    float a[8] = {};
#pragma unroll
    for (int ii = 0; ii < 22; ++ii) { const int i = r + 3 * ii; if (i >= 64) break;
      const v4u w = *(const v4u*)(wo + (size_t)(j * 64 + i) * D + c * 8); const float x = o[i];
#pragma unroll
      for (int k = 0; k < 4; ++k) { a[2 * k] += x * lo16(w[k]); a[2 * k + 1] += x * hi16(w[k]); }
    }
#pragma unroll
    for (int k = 0; k < 8; ++k) acc[r][c * 8 + k] = a[k];
  }
  __syncthreads();
  for (int i = tid; i < D; i += 256) part[(size_t)j * D + i] = acc[0][i] + acc[1][i] + acc[2][i];
}

__global__ void __launch_bounds__(256) k_mlp(const float* xprev, const float* partin, int np, float* xout, const u16* wg_, const u16* wu, const u16* wd, float* part) {
  __shared__ float xs[D], ln[D], red[8], accg[64][33], accu[64][33], hs[32], acc[3][D];
  const int j = blockIdx.x, tid = threadIdx.x;
  gather_x(nullptr, xprev, partin, np, xs);
  if (j == 0) for (int i = tid; i < D; i += 256) xout[i] = xs[i];
  layernorm_block(xs, ln, red);
  {
    const int r = tid >> 2, c = tid & 3, col0 = j * 32 + c * 8;
    float a[8] = {}, b[8] = {};
#pragma unroll
    for (int ii = 0; ii < 9; ++ii) { const int i = r + 64 * ii;
      const v4u w = *(const v4u*)(wg_ + (size_t)i * F + col0), u = *(const v4u*)(wu + (size_t)i * F + col0); const float l = ln[i];
#pragma unroll
      for (int k = 0; k < 4; ++k) { a[2 * k] += l * lo16(w[k]); a[2 * k + 1] += l * hi16(w[k]); b[2 * k] += l * lo16(u[k]); b[2 * k + 1] += l * hi16(u[k]); }
    }
#pragma unroll
    for (int k = 0; k < 8; ++k) { accg[r][c * 8 + k] = a[k]; accu[r][c * 8 + k] = b[k]; }
  }
  __syncthreads();
  if (tid < 32) { float sg = 0.f, su = 0.f; for (int r = 0; r < 64; ++r) { sg += accg[r][tid]; su += accu[r][tid]; }
    sg = rbf(sg); su = rbf(su); const float gt = sg > 0.f ? sg : sg * 0.f; hs[tid] = rbf(gt * su); }
  __syncthreads();
  const int c = tid % 72, r = tid / 72;
  if (r < 3) {
    float a[8] = {};
#pragma unroll
    for (int ii = 0; ii < 11; ++ii) { const int i = r + 3 * ii; if (i >= 32) break;
      const v4u w = *(const v4u*)(wd + (size_t)(j * 32 + i) * D + c * 8); const float x = hs[i];
#pragma unroll
      for (int k = 0; k < 4; ++k) { a[2 * k] += x * lo16(w[k]); a[2 * k + 1] += x * hi16(w[k]); }
    }
#pragma unroll
    for (int k = 0; k < 8; ++k) acc[r][c * 8 + k] = a[k];
  }
  __syncthreads();
  for (int i = tid; i < D; i += 256) part[(size_t)j * D + i] = acc[0][i] + acc[1][i] + acc[2][i];
}

__global__ void __launch_bounds__(64) k_final(const float* xprev, const float* part, int np, u16* out) {
  const int i = blockIdx.x * 64 + threadIdx.x;
  float s = 0.f;
  for (int p0 = 0; p0 < np; p0 += 16) { float t[16];
#pragma unroll
    for (int u = 0; u < 16; ++u) t[u] = part[(size_t)(p0 + u) * D + i];
#pragma unroll
    for (int u = 0; u < 16; ++u) s += t[u]; }
  out[i] = f2bf(xprev[i] + rbf(s));
}

extern "C" void kernel_launch(void* const* d_in, const int* in_sizes, int n_in, void* d_out, int out_size, void* d_ws, size_t ws_size, hipStream_t stream) {
  const u16 *x = (const u16*)d_in[0], *K = (const u16*)d_in[1], *V = (const u16*)d_in[2], *wq = (const u16*)d_in[3], *wo = (const u16*)d_in[4];
  const u16 *wg = (const u16*)d_in[6], *wu = (const u16*)d_in[7], *wd = (const u16*)d_in[8];
  char* ws = (char*)d_ws;
  u16* q = (u16*)ws; float* pml = (float*)(ws + 16384); float* po = (float*)(ws + 32768);
  float* part1 = (float*)(ws + (1 << 20)); float* part2 = (float*)(ws + (2 << 20));
  float *xa = (float*)(ws + (3 << 20)), *xb = xa + 1024, *xc = xa + 2048, *xd = xa + 3072;
  k_q<<<64, 256, 0, stream>>>(x, nullptr, nullptr, 0, xa, wq, q);
  k_attn<K1, V1><<<256, AW * 64, 0, stream>>>(q, K, V, pml, po);
  k_oproj<<<64, 256, 0, stream>>>(pml, po, wo, part1);
  k_mlp<<<144, 256, 0, stream>>>(xa, part1, 64, xb, wg, wu, wd, part2);
  k_q<<<64, 256, 0, stream>>>(nullptr, xb, part2, 144, xc, wq, q);
  k_attn<K2, V2><<<256, AW * 64, 0, stream>>>(q, K, V, pml, po);
  k_oproj<<<64, 256, 0, stream>>>(pml, po, wo, part1);
  k_mlp<<<144, 256, 0, stream>>>(xc, part1, 64, xd, wg, wu, wd, part2);
  k_final<<<9, 64, 0, stream>>>(xd, part2, 144, (u16*)d_out);
}
```

```cpp
#include <hip/hip_runtime.h>
#include <hip/hip_bf16.h>
#include <cstdint>

#define DIAG 0

namespace {

typedef unsigned v4u __attribute__((ext_vector_type(4)));
typedef unsigned v2u __attribute__((ext_vector_type(2)));
typedef float f32x4 __attribute__((ext_vector_type(4)));
typedef float f32x16 __attribute__((ext_vector_type(16)));
typedef float f32x32 __attribute__((ext_vector_type(32)));
typedef __bf16 bf16x2 __attribute__((ext_vector_type(2)));
typedef unsigned short u16;
typedef unsigned long long u64;
#define LAS __attribute__((address_space(3)))
#define GAS __attribute__((address_space(1)))

constexpr int D = 576, DH = 128, F = 4608, QD = 4096;
constexpr int NCU = 256;
constexpr float C2 = 0.088388347648318440f * 1.4426950408889634f;

constexpr int NLDS = 14;
constexpr int T_W0 = 36;
constexpr int T_LDS = 38;
constexpr int T_KEEP = T_LDS + NLDS;
constexpr int T_Q = T_KEEP + 12;
constexpr int NQ = 256 - T_Q;
#define NL2TILES 16
constexpr int NL2 = NL2TILES;
#define L2POS 12
constexpr int NPRE = L2POS;

constexpr int L_TILES = 0;
constexpr int L_WO = NLDS * 8192;
constexpr int L_WD = L_WO + 16 * D * 2;
constexpr int L_MRG = L_WD + 18 * D * 2;
constexpr int L_LN = L_MRG + 4 * 260 * 4;
constexpr int L_Q = L_LN + D * 2;
constexpr int L_O = L_Q + 512;
constexpr int L_H = L_O + 64;
constexpr int L_QP = L_H + 64;
constexpr int L_CTL = L_QP + 256;
constexpr int L_END = L_CTL + 128;
constexpr int L_IMG = L_WO;
static_assert(L_IMG + 2 * D * 36 <= L_LN, "gate/up image");
constexpr int LDS_BYTES = 163840;
static_assert(L_END <= LDS_BYTES, "LDS budget");

constexpr int W_EPOCH = 0;
constexpr int NRED = 36, NREP = 8, XREP = 2560;
constexpr int W_X = 4096;
constexpr int W_Q = 32768;
constexpr int W_REC = 65536, REC = 7168;
constexpr int R_O = 0, R_Y = 2048, R_ML = R_Y + 4608;
constexpr int W_DIAG = W_REC + NCU * REC;
constexpr int W_END = W_DIAG + NCU * 32 * 8;
constexpr int AUX_SC1 = 16;

#define SL0 8
#define SL_Y 8
#define SL_X 4
#define DLY_Y1 90
#define DLY_X1 220
#define DLY_Y2 90
#define DLY_X2 220

struct Params {
  const u16 *x, *K, *V, *wq, *wo, *wg, *wu, *wd;
  u16* out;
  unsigned char* ws;
};

__device__ __forceinline__ float rbf(float f) { return (float)(__bf16)f; }
__device__ __forceinline__ unsigned f2bf(float f) { return (unsigned)__builtin_bit_cast(u16, (__bf16)f); }
__device__ __forceinline__ float bf2f(unsigned b) { return __builtin_bit_cast(float, b << 16); }
__device__ __forceinline__ float asf(unsigned u) { return __builtin_bit_cast(float, u); }
__device__ __forceinline__ unsigned asu(float f) { return __builtin_bit_cast(unsigned, f); }
__device__ __forceinline__ float dot2(unsigned a, unsigned b, float c) {
  return __builtin_amdgcn_fdot2_f32_bf16(__builtin_bit_cast(bf16x2, a), __builtin_bit_cast(bf16x2, b), c, false);
}
template <int CTRL> __device__ __forceinline__ float dpp(float x) {
  return asf((unsigned)__builtin_amdgcn_mov_dpp((int)asu(x), CTRL, 0xf, 0xf, true));
}
constexpr int XOR1 = 0xB1, XOR2 = 0x4E, ROR4 = 0x124, ROR8 = 0x128;
__device__ __forceinline__ float swap16_add(float a, float b) {
  auto s = __builtin_amdgcn_permlane16_swap(asu(a), asu(b), false, false);
  return asf(s[0]) + asf(s[1]);
}
__device__ __forceinline__ float swap32_add(float a, float b) {
  auto s = __builtin_amdgcn_permlane32_swap(asu(a), asu(b), false, false);
  return asf(s[0]) + asf(s[1]);
}
__device__ __forceinline__ float xrow_max(float x) {
  auto s = __builtin_amdgcn_permlane16_swap(asu(x), asu(x), false, false);
  x = fmaxf(asf(s[0]), asf(s[1]));
  auto t = __builtin_amdgcn_permlane32_swap(asu(x), asu(x), false, false);
  return fmaxf(asf(t[0]), asf(t[1]));
}
__device__ __forceinline__ float xrow_sum(float x) { x = swap16_add(x, x); return swap32_add(x, x); }
__device__ __forceinline__ float wave_sum(float x) {
  x += dpp<XOR1>(x); x += dpp<XOR2>(x); x += dpp<ROR4>(x); x += dpp<ROR8>(x);
  return xrow_sum(x);
}

__device__ __forceinline__ int fresh_lane() { int l; asm volatile("v_mbcnt_lo_u32_b32 %0, -1, 0\n\tv_mbcnt_hi_u32_b32 %0, -1, %0" : "=v"(l)); return l; }
__device__ __forceinline__ int fresh(int x) { asm volatile("" : "+s"(x)); return x; }
__device__ __forceinline__ const Params* kargs() {
  auto k = (const __attribute__((address_space(4))) Params*)__builtin_amdgcn_kernarg_segment_ptr();
  asm volatile("" : "+s"(k));
  return (const Params*)k;
}
#define BAR() asm volatile("s_waitcnt lgkmcnt(0)\n\ts_barrier" ::: "memory")
template <int N> __device__ __forceinline__ void waitvm() { asm volatile("s_waitcnt vmcnt(%c0)" :: "i"(N) : "memory"); }

template <int S> struct Slot { static constexpr bool acc = S >= 6; static constexpr int base = S >= 6 ? 32 * (S - 6) : 64 + 32 * S; };

#define LD1(P, R, VOFF, RS, OFF, POL) "buffer_load_dwordx4 " P "[%c0+" #R ":%c0+" #R "+3], " VOFF ", " RS ", %5 offen offset:" #OFF POL "\n\t"
#define LD8(P, POL) LD1(P, 0, "%1", "%3", 0, POL) LD1(P, 4, "%1", "%3", 64, POL) LD1(P, 8, "%1", "%3", 128, POL) LD1(P, 12, "%1", "%3", 192, POL) \
                    LD1(P, 16, "%2", "%4", 0, POL) LD1(P, 20, "%2", "%4", 1024, POL) LD1(P, 24, "%2", "%4", 2048, POL) LD1(P, 28, "%2", "%4", 3072, POL)
template <int S, bool NT> __device__ __forceinline__ void tile_load(int vk, int vv, __amdgpu_buffer_rsrc_t rk, __amdgpu_buffer_rsrc_t rv, int soff) {
  if constexpr (Slot<S>::acc) {
    if constexpr (NT) asm volatile(LD8("a", " nt") :: "i"(Slot<S>::base), "v"(vk), "v"(vv), "s"(rk), "s"(rv), "s"(soff) : "memory");
    else asm volatile(LD8("a", "") :: "i"(Slot<S>::base), "v"(vk), "v"(vv), "s"(rk), "s"(rv), "s"(soff) : "memory");
  } else {
    if constexpr (NT) asm volatile(LD8("v", " nt") :: "i"(Slot<S>::base), "v"(vk), "v"(vv), "s"(rk), "s"(rv), "s"(soff) : "memory");
    else asm volatile(LD8("v", "") :: "i"(Slot<S>::base), "v"(vk), "v"(vv), "s"(rk), "s"(rv), "s"(soff) : "memory");
  }
}
__device__ __forceinline__ void dma1k(int voff, __amdgpu_buffer_rsrc_t rs, int soff, int m0v) {
  asm volatile("s_mov_b32 m0, %3\n\ts_nop 0\n\tbuffer_load_dwordx4 %0, %1, %2 offen nt lds" :: "v"(voff), "s"(rs), "s"(soff), "s"(m0v) : "memory");
}
__device__ __forceinline__ void tile_dma(int vk, int vv, __amdgpu_buffer_rsrc_t rk, __amdgpu_buffer_rsrc_t rv, int soff, int ldsb) {
#pragma unroll
  for (int i = 0; i < 4; ++i) dma1k(vk, rk, soff + 64 * i, ldsb + 1024 * i);
#pragma unroll
  for (int i = 0; i < 4; ++i) dma1k(vv, rv, soff + 1024 * i, ldsb + 4096 + 1024 * i);
}
#define DR1(R, OFF) "ds_read_b128 v[%c0+" #R ":%c0+" #R "+3], %1 offset:" #OFF "\n\t"
template <int S> __device__ __forceinline__ void tile_from_lds(int addr) {
  static_assert(!Slot<S>::acc, "streaming slots only");
  asm volatile(DR1(0, 0) DR1(4, 1024) DR1(8, 2048) DR1(12, 3072) DR1(16, 4096) DR1(20, 5120) DR1(24, 6144) DR1(28, 7168) "s_waitcnt lgkmcnt(0)"
               :: "i"(Slot<S>::base), "v"(addr) : "memory");
}

struct Att { v4u q0, q1, q2, q3; unsigned k0, k1, k2, k3; f32x4 acc; float m, l; };

#define QK4(P) "v_mfma_f32_16x16x32_bf16 %0, " P "[%c5:%c5+3], %1, 0\n\t" \
               "v_mfma_f32_16x16x32_bf16 %0, " P "[%c5+4:%c5+7], %2, %0\n\t" \
               "v_mfma_f32_16x16x32_bf16 %0, " P "[%c5+8:%c5+11], %3, %0\n\t" \
               "v_mfma_f32_16x16x32_bf16 %0, " P "[%c5+12:%c5+15], %4, %0\n\ts_nop 7"
#define PV1(P) "s_nop 1\n\tv_mfma_f32_16x16x32_bf16 %0, " P "[%c2:%c2+3], %1, %0\n\ts_nop %c3"
template <int S> __device__ __forceinline__ void tile_compute(Att& a) {
  f32x4 s;
  if constexpr (Slot<S>::acc) asm volatile(QK4("a") : "=&v"(s) : "v"(a.q0), "v"(a.q1), "v"(a.q2), "v"(a.q3), "i"(Slot<S>::base));
  else asm volatile(QK4("v") : "=&v"(s) : "v"(a.q0), "v"(a.q1), "v"(a.q2), "v"(a.q3), "i"(Slot<S>::base));
  const float mx = xrow_max(fmaxf(fmaxf(s[0], s[1]), fmaxf(s[2], s[3])));
  const float mn = fmaxf(a.m, mx * C2);
  const float alpha = __builtin_amdgcn_exp2f(a.m - mn);
  a.m = mn;
  a.acc *= alpha;
  float ps = 0.f;
#pragma unroll
  for (int t = 0; t < 4; ++t) {
    const float p = __builtin_amdgcn_exp2f(fmaf(s[t], C2, -mn));
    ps += p;
    bf16x2 pb = {(__bf16)p, (__bf16)p};
    const unsigned pp = __builtin_bit_cast(unsigned, pb);
    const v4u b = {pp & a.k0, pp & a.k1, pp & a.k2, pp & a.k3};
    if constexpr (Slot<S>::acc) asm volatile(PV1("a") : "+v"(a.acc) : "v"(b), "i"(Slot<S>::base + 16 + 4 * t), "i"(t == 3 ? 7 : 0));
    else asm volatile(PV1("v") : "+v"(a.acc) : "v"(b), "i"(Slot<S>::base + 16 + 4 * t), "i"(t == 3 ? 7 : 0));
  }
  a.l = fmaf(a.l, alpha, ps);
}

__device__ __forceinline__ int lds_claim(int addr) {
  int t; unsigned tmp; const unsigned one = 1;
  asm volatile("s_mov_b64 exec, 1\n\tds_add_rtn_u32 %1, %2, %3\n\ts_waitcnt lgkmcnt(0)\n\tv_readfirstlane_b32 %0, %1\n\ts_mov_b64 exec, -1"
               : "=s"(t), "=&v"(tmp) : "v"(addr), "v"(one) : "memory");
  return t;
}
constexpr unsigned SEL_LO = 0x05040100u, SEL_HI = 0x07060302u;
__device__ __forceinline__ unsigned perm2(unsigned a, unsigned b, unsigned sel) { return __builtin_amdgcn_perm(b, a, sel); }

constexpr unsigned SPIN_TICKS = 20000000u;
template <int SLEEP> __device__ __forceinline__ bool spin_more(u64 t0) {
  __builtin_amdgcn_s_sleep(SLEEP);
  return (unsigned)(__builtin_amdgcn_s_memrealtime() - t0) < SPIN_TICKS;
}
__device__ __forceinline__ void pause_until(u64 t0, unsigned ticks) {
  while ((unsigned)(__builtin_amdgcn_s_memrealtime() - t0) < ticks) __builtin_amdgcn_s_sleep(1);
}

__device__ __forceinline__ int piece_col(int P) { return P < 96 ? 12 * (P >> 1) + 4 * (P & 1) : 12 * (P - 96) + 8; }

__device__ __forceinline__ void layernorm_to_lds(const v2u (&xp)[3], LAS unsigned char* lds, int lane) {
  const bool live = lane < 48;
  float x[12];
#pragma unroll
  for (int k = 0; k < 3; ++k) {
    x[4 * k] = asf(xp[k][0] << 16); x[4 * k + 1] = asf(xp[k][0] & 0xffff0000u); x[4 * k + 2] = asf(xp[k][1] << 16); x[4 * k + 3] = asf(xp[k][1] & 0xffff0000u);
  }
  float s = 0.f;
#pragma unroll
  for (int i = 0; i < 12; ++i) s += x[i];
  const float mean = wave_sum(live ? s : 0.f) * (1.f / D);
  float v = 0.f;
#pragma unroll
  for (int i = 0; i < 12; ++i) { const float d = x[i] - mean; v = fmaf(d, d, v); }
  const float var = rbf(wave_sum(live ? v : 0.f) * (1.f / D));
  const float mb = rbf(mean), inv = __builtin_amdgcn_rcpf(rbf(__builtin_amdgcn_sqrtf(rbf(var + 1e-5f))));
  if (live) {
#pragma unroll
    for (int k = 0; k < 3; ++k) {
      bf16x2 lo = {(__bf16)(rbf(x[4 * k] - mb) * inv), (__bf16)(rbf(x[4 * k + 1] - mb) * inv)};
      bf16x2 hi = {(__bf16)(rbf(x[4 * k + 2] - mb) * inv), (__bf16)(rbf(x[4 * k + 3] - mb) * inv)};
      *(LAS v2u*)(lds + L_LN + 2 * piece_col(lane + 48 * k)) = v2u{__builtin_bit_cast(unsigned, lo), __builtin_bit_cast(unsigned, hi)};
    }
  }
}

template <int NP, int NROWPAIRS>
__device__ __forceinline__ void rows_gemv_publish(LAS unsigned char* lds, int wbase, int inbase, int wp, int lane,
                                                  __amdgpu_buffer_rsrc_t rw, int ybase, unsigned tag) {
  const int rg = lane >> 4, c = lane & 15;
  float acc[12];
#pragma unroll
  for (int k = 0; k < 12; ++k) acc[k] = 0.f;
#pragma unroll
  for (int i = 0; i < NP; ++i) {
    const int p = rg + 4 * i, pw = p < NROWPAIRS ? p : NROWPAIRS - 1;
    const unsigned in2 = *(LAS unsigned*)(lds + inbase + 4 * p);
    LAS v2u* r0 = (LAS v2u*)(lds + wbase + (2 * pw) * (D * 2) + 384 * wp + 24 * c);
    LAS v2u* r1 = (LAS v2u*)(lds + wbase + (2 * pw + 1) * (D * 2) + 384 * wp + 24 * c);
#pragma unroll
    for (int j = 0; j < 3; ++j) {
      const v2u a = r0[j], b = r1[j];
      acc[4 * j + 0] = dot2(perm2(a[0], b[0], SEL_LO), in2, acc[4 * j + 0]);
      acc[4 * j + 1] = dot2(perm2(a[0], b[0], SEL_HI), in2, acc[4 * j + 1]);
      acc[4 * j + 2] = dot2(perm2(a[1], b[1], SEL_LO), in2, acc[4 * j + 2]);
      acc[4 * j + 3] = dot2(perm2(a[1], b[1], SEL_HI), in2, acc[4 * j + 3]);
    }
  }
  const float y0 = swap32_add(swap16_add(acc[0], acc[2]), swap16_add(acc[4], acc[6]));
  const float y1 = swap32_add(swap16_add(acc[1], acc[3]), swap16_add(acc[5], acc[7]));
  const float y2 = swap32_add(swap16_add(acc[8], acc[9]), swap16_add(acc[10], acc[11]));
  const int idx = 64 * wp + 4 * c + rg;
  __builtin_amdgcn_raw_buffer_store_b128(v4u{asu(y0), tag, asu(y1), tag}, rw, ybase + 16 * idx, 0, AUX_SC1);
  __builtin_amdgcn_raw_buffer_store_b64(v2u{asu(y2), tag}, rw, ybase + 3072 + 8 * idx, 0, AUX_SC1);
}

template <int SLEEP>
__device__ __forceinline__ void gather_y(LAS unsigned char* lds, __amdgpu_buffer_rsrc_t rw, int rho, int wave, int lane, unsigned tag, u64 t0) {
  const int off = W_REC + (64 * wave + (lane >> 3)) * REC + R_Y + 128 * rho + 16 * (lane & 7);
  float s0, s1;
  bool ok;
  do {
    v4u a[8];
#pragma unroll
    for (int k = 0; k < 8; ++k) a[k] = __builtin_amdgcn_raw_buffer_load_b128(rw, off, k * 8 * REC, AUX_SC1);
    ok = true; s0 = 0.f; s1 = 0.f;
#pragma unroll
    for (int k = 0; k < 8; ++k) { ok &= (a[k][1] == tag) & (a[k][3] == tag); s0 += asf(a[k][0]); s1 += asf(a[k][2]); }
  } while (!__all(ok) && spin_more<SLEEP>(t0));
  s0 += dpp<ROR8>(s0); s1 += dpp<ROR8>(s1);
  s0 = xrow_sum(s0); s1 = xrow_sum(s1);
  if (lane < 8) *(LAS v2u*)(lds + L_QP + 64 * wave + 8 * lane) = v2u{asu(s0), asu(s1)};
}
__device__ __forceinline__ unsigned finish_x(LAS unsigned char* lds, int lane) {
  const int j = lane & 7;
  float s0 = 0.f, s1 = 0.f;
#pragma unroll
  for (int w = 0; w < 4; ++w) { const v2u t = *(LAS v2u*)(lds + L_QP + 64 * w + 8 * j); s0 += asf(t[0]); s1 += asf(t[1]); }
  LAS unsigned* res = (LAS unsigned*)(lds + L_CTL + 64) + j;
  const unsigned r = *res;
  bf16x2 xb = {(__bf16)(asf(r << 16) + rbf(s0)), (__bf16)(asf(r & 0xffff0000u) + rbf(s1))};
  const unsigned xn = __builtin_bit_cast(unsigned, xb);
  asm volatile("s_waitcnt lgkmcnt(0)" ::: "memory");
  *res = xn;
  return xn;
}
template <int SLEEP>
__device__ __forceinline__ void gather_x(__amdgpu_buffer_rsrc_t rw, int cu, int lane, unsigned tag, v2u (&xp)[3], u64 t0) {
  v4u g[3] = {};
  const int off = W_X + (cu & (NREP - 1)) * XREP + 16 * (lane < 48 ? lane : 47);
  bool ok = false;
  do {
    if (!ok) {
#pragma unroll
      for (int k = 0; k < 3; ++k) g[k] = __builtin_amdgcn_raw_buffer_load_b128(rw, off + 768 * k, 0, AUX_SC1);
      ok = (g[0][1] == tag) & (g[0][3] == tag) & (g[1][1] == tag) & (g[1][3] == tag) & (g[2][1] == tag) & (g[2][3] == tag);
    }
  } while (!__all(ok) && spin_more<SLEEP>(t0));
#pragma unroll
  for (int k = 0; k < 3; ++k) xp[k] = v2u{g[k][0], g[k][2]};
}

#define STAMP(i) do {} while (0)

template <int K> __device__ __forceinline__ void wq_load_one(int voff, __amdgpu_buffer_rsrc_t rs) {
  asm volatile("buffer_load_dwordx4 v[%c0:%c0+3], %1, %2, %3 offen" :: "i"(136 + 4 * K), "v"(voff), "s"(rs), "s"(K * QD * 2) : "memory");
}
__device__ __forceinline__ void wq_load(const u16* wq, int kap, int wp, int lane) {
  __amdgpu_buffer_rsrc_t rs = __builtin_amdgcn_make_buffer_rsrc((void*)wq, 0, D * QD * 2, 0x00020000);
  const int voff = (192 * wp + 6 * (lane >> 1)) * (QD * 2) + 32 * kap + 16 * (lane & 1);
  wq_load_one<0>(voff, rs); wq_load_one<1>(voff, rs); wq_load_one<2>(voff, rs); wq_load_one<3>(voff, rs); wq_load_one<4>(voff, rs); wq_load_one<5>(voff, rs);
}
__device__ __forceinline__ void dma_dword(int voff, __amdgpu_buffer_rsrc_t rs, int soff, int m0v) {
  asm volatile("s_mov_b32 m0, %3\n\ts_nop 0\n\tbuffer_load_dword %0, %1, %2 offen lds" :: "v"(voff), "s"(rs), "s"(soff), "s"(m0v) : "memory");
}
constexpr int wq_reg(int j) { return j < 10 ? 214 + j : 48 + (j - 10); }
template <int DST0, int DST1, int RA, int RB> __device__ __forceinline__ void pair_regs() {
  asm volatile("v_perm_b32 v%c0, v%c3, v%c2, %4\n\tv_perm_b32 v%c1, v%c3, v%c2, %5" :: "i"(DST0), "i"(DST1), "i"(RA), "i"(RB), "s"(SEL_LO), "s"(SEL_HI));
}
template <int DST> __device__ __forceinline__ void pair_vals(unsigned a, unsigned b) {
  asm volatile("v_perm_b32 v%c0, %2, %1, %3\n\tv_perm_b32 v[%c0+1], %2, %1, %4" :: "i"(DST), "v"(a), "v"(b), "s"(SEL_LO), "s"(SEL_HI));
}
template <int I> __device__ __forceinline__ void gu_repair(LAS unsigned* img) {
  const unsigned a0 = img[18 * I], a1 = img[18 * I + 1], a2 = img[18 * I + 2], b0 = img[18 * I + 9], b1 = img[18 * I + 10], b2 = img[18 * I + 11];
  pair_vals<160 + 6 * I>(a0, b0); pair_vals<162 + 6 * I>(a1, b1); pair_vals<164 + 6 * I>(a2, b2);
}
template <int I> __device__ __forceinline__ void wq_repair() {
  pair_regs<wq_reg(8 * I + 0), wq_reg(8 * I + 1), 136 + 8 * I + 0, 140 + 8 * I + 0>();
  pair_regs<wq_reg(8 * I + 2), wq_reg(8 * I + 3), 136 + 8 * I + 1, 140 + 8 * I + 1>();
  pair_regs<wq_reg(8 * I + 4), wq_reg(8 * I + 5), 136 + 8 * I + 2, 140 + 8 * I + 2>();
  pair_regs<wq_reg(8 * I + 6), wq_reg(8 * I + 7), 136 + 8 * I + 3, 140 + 8 * I + 3>();
}
template <int R0, int R1> __device__ __forceinline__ void dot_regs(float& a0, float& a1, unsigned in2) {
  asm volatile("v_dot2c_f32_bf16 %0, v%c2, %4\n\tv_dot2c_f32_bf16 %1, v%c3, %4" : "+v"(a0), "+v"(a1) : "i"(R0), "i"(R1), "v"(in2));
}
template <int I> __device__ __forceinline__ void gu_pair(float (&acc)[6], unsigned in2) {
  dot_regs<160 + 6 * I + 0, 160 + 6 * I + 1>(acc[0], acc[1], in2);
  dot_regs<160 + 6 * I + 2, 160 + 6 * I + 3>(acc[2], acc[3], in2);
  dot_regs<160 + 6 * I + 4, 160 + 6 * I + 5>(acc[4], acc[5], in2);
}
template <int I> __device__ __forceinline__ void wq_pair(float (&acc)[8], unsigned in2) {
  dot_regs<wq_reg(8 * I + 0), wq_reg(8 * I + 1)>(acc[0], acc[1], in2);
  dot_regs<wq_reg(8 * I + 2), wq_reg(8 * I + 3)>(acc[2], acc[3], in2);
  dot_regs<wq_reg(8 * I + 4), wq_reg(8 * I + 5)>(acc[4], acc[5], in2);
  dot_regs<wq_reg(8 * I + 6), wq_reg(8 * I + 7)>(acc[6], acc[7], in2);
}

struct Stream { int st0, st1, st2, seq0, seq1, seq2, ops, nl, ldsa; };

__global__ void __attribute__((amdgpu_num_vgpr(48))) __launch_bounds__(256, 1) fused_decode(Params p) {
  extern __shared__ __attribute__((aligned(16))) unsigned char lds_raw[];
  LAS unsigned char* lds = (LAS unsigned char*)lds_raw;
  asm volatile("" ::: "v255", "a255");
  f32x32 pin0; f32x16 pin1;
  asm volatile("" : "=a"(pin0), "=a"(pin1));
  int wave = __builtin_amdgcn_readfirstlane(threadIdx.x >> 6), cu = blockIdx.x, lane = fresh_lane();
#define REFRESH() do { lane = fresh_lane(); cu = fresh(cu); wave = fresh(wave); } while (0)
#define grp (cu >> 4)
#define sp (cu & 15)
#define wp (wave - 1)
#define mu ((cu & 7) * 32 + (cu >> 3))
#define kvb (cu << 20)
#define rec (W_REC + cu * REC)
#define rho ((cu * 9363) >> 16)
#define isred (cu == 7 * rho && cu < 7 * NRED)
  const __amdgpu_buffer_rsrc_t rk = __builtin_amdgcn_make_buffer_rsrc((void*)p.K, 0, 1 << 28, 0x00020000);
  const __amdgpu_buffer_rsrc_t rv = __builtin_amdgcn_make_buffer_rsrc((void*)p.V, 0, 1 << 28, 0x00020000);
  const __amdgpu_buffer_rsrc_t rw = __builtin_amdgcn_make_buffer_rsrc((void*)p.ws, 0, W_END, 0x00020000);
  volatile LAS unsigned* ctl = (volatile LAS unsigned*)(lds + L_CTL);
  STAMP(wave == 0 ? 0 : 31);

  if (wave == 0) {
    const v2u e = __builtin_amdgcn_raw_buffer_load_b64(rw, W_EPOCH, 0, AUX_SC1);
    v2u xp[3];
#pragma unroll
    for (int k = 0; k < 3; ++k) xp[k] = *(const GAS v2u*)(p.x + piece_col((lane < 48 ? lane : 47) + 48 * k));
    const unsigned xr = *(const GAS unsigned*)(p.x + piece_col(4 * (isred ? rho : 0) + ((lane & 7) >> 1)) + 2 * (lane & 1));
#pragma unroll
    for (int k = 0; k < 7; ++k) __builtin_amdgcn_raw_buffer_store_b128(v4u{0, 0, 0, 0}, rw, rec + 1024 * k + 16 * lane, 0, AUX_SC1);
    if (isred) __builtin_amdgcn_raw_buffer_store_b64(v2u{0, 0}, rw, W_X + (lane >> 3) * XREP + 64 * rho + 8 * (lane & 7), 0, AUX_SC1);
    if (lane < 4) ctl[lane] = 0u;
    if (lane < 16) *(LAS unsigned*)(lds + L_H + 4 * lane) = 0u;
    if (lane < 8) ctl[16 + lane] = xr;
    if (lane == 0) { ctl[4] = e[0]; ctl[5] = e[1]; }
    layernorm_to_lds(xp, lds, lane);
  } else {
    wq_load(p.wq, mu, wp, lane);
    const int vv = lane * 16, vk = (4 * (lane & 3) + ((lane >> 2) & 3)) * 256 + (lane >> 4) * 16;
#pragma unroll
    for (int k = 0; k < 3; ++k) tile_dma(vk, vv, rk, rv, kvb + (T_LDS + wave + 4 * k) * 4096, L_TILES + (wave + 4 * k) * 8192);
  }

  Stream s;
  s.ops = 0;

  for (int layer = 0; layer < 2; ++layer) {
    const unsigned tg = 4 * layer;
    const int qctr = L_CTL + 4 * layer;
    BAR();
    REFRESH();
    if (wave != 0) {
      if (layer == 0) { waitvm<24>(); wq_repair<0>(); wq_repair<1>(); wq_repair<2>(); }
      float acc[8];
#pragma unroll
      for (int k = 0; k < 8; ++k) acc[k] = 0.f;
      LAS unsigned* ln2 = (LAS unsigned*)(lds + L_LN) + 96 * wp + 3 * (lane >> 1);
      const unsigned i0 = ln2[0], i1 = ln2[1], i2 = ln2[2];
      wq_pair<0>(acc, i0); wq_pair<1>(acc, i1); wq_pair<2>(acc, i2);
      asm volatile("s_nop 2" : "+v"(acc[0]), "+v"(acc[1]), "+v"(acc[2]), "+v"(acc[3]), "+v"(acc[4]), "+v"(acc[5]), "+v"(acc[6]), "+v"(acc[7]));
      float u[2];
#pragma unroll
      for (int k = 0; k < 2; ++k) {
        float t = swap16_add(swap32_add(acc[k], acc[k + 4]), swap32_add(acc[k + 2], acc[k + 6]));
        t += dpp<ROR8>(t); t += dpp<ROR4>(t); t += dpp<XOR2>(t);
        u[k] = t;
      }
      if ((lane & 14) == 0) {
        LAS float* qp = (LAS float*)(lds + L_QP) + 16 * wp + 8 * (lane & 1) + 4 * (lane >> 5) + 2 * ((lane >> 4) & 1);
        qp[0] = u[0]; qp[1] = u[1];
      }
    }
    BAR();
    REFRESH();
    STAMP(wave == 0 ? 1 + 12 * layer : 31);
    auto qtile = [&](int t) -> int {
      if (layer == 0 || t < NPRE) return T_Q + t;
      return t < NPRE + NL2 ? T_Q + (NQ - NL2) + (t - NPRE) : T_Q + t - NL2;
    };
#define ISSUE_Q(S, t) do { const int so_ = kv0 + qtile(t) * 4096; \
      if (layer == 0 && (t) >= NQ - NL2) tile_load<S, false>(vk, vv, rk, rv, so_); else tile_load<S, true>(vk, vv, rk, rv, so_); s.ops += 8; } while (0)
    if (wave == 0) {
      const u64 epoch = ((u64)(unsigned)__builtin_amdgcn_readfirstlane(ctl[5]) << 32) | (unsigned)__builtin_amdgcn_readfirstlane(ctl[4]);
      const u64 tag = (epoch + 1 + layer) << 16;
      if (lane < 16) {
        LAS float* qp = (LAS float*)(lds + L_QP) + lane;
        const u64 g = tag | f2bf((qp[0] + qp[16]) + qp[32]);
        if (layer == 0) waitvm<0>();
        __builtin_amdgcn_raw_buffer_store_b64(v2u{(unsigned)g, (unsigned)(g >> 32)}, rw, W_Q + mu * 128 + 8 * lane, 0, AUX_SC1);
      }
      const unsigned tlo = (unsigned)tag >> 16, thi = (unsigned)(tag >> 32);
      {
        v4u g0 = {}, g1 = {};
        const u64 t0 = __builtin_amdgcn_s_memrealtime();
        bool ok = false;
        do {
          if (!ok) {
            g0 = __builtin_amdgcn_raw_buffer_load_b128(rw, W_Q + 2048 * grp + 32 * lane, 0, AUX_SC1);
            g1 = __builtin_amdgcn_raw_buffer_load_b128(rw, W_Q + 2048 * grp + 32 * lane + 16, 0, AUX_SC1);
            ok = ((g0[0] >> 16) == tlo) & (g0[1] == thi) & ((g0[2] >> 16) == tlo) & (g0[3] == thi) &
                 ((g1[0] >> 16) == tlo) & (g1[1] == thi) & ((g1[2] >> 16) == tlo) & (g1[3] == thi);
          }
        } while (!__all(ok) && spin_more<SL0>(t0));
        *(LAS v2u*)(lds + L_Q + 8 * lane) = v2u{perm2(g0[0], g0[2], SEL_LO), perm2(g1[0], g1[2], SEL_LO)};
        asm volatile("s_waitcnt lgkmcnt(0)" ::: "memory");
        ctl[2] = layer + 1;
        STAMP(layer == 0 ? 24 : 31);
      }
      if (layer == 0) {
        const u64 t0 = __builtin_amdgcn_s_memrealtime();
        bool ok = false;
        do {
          if (!ok) {
            ok = true;
#pragma unroll
            for (int k = 0; k < 4; ++k) {
              const v2u g = __builtin_amdgcn_raw_buffer_load_b64(rw, W_Q + 128 * (lane + 64 * k), 0, AUX_SC1);
              ok &= ((g[0] >> 16) == tlo) & (g[1] == thi);
            }
          }
        } while (!__all(ok) && spin_more<SL0>(t0));
      }
    }
    STAMP(wave == 0 ? 2 + 12 * layer : 31);
    REFRESH();
    const int n16 = lane & 15, gq = lane >> 4, kv0 = kvb;
    const int vk = (4 * (n16 & 3) + (n16 >> 2)) * 256 + gq * 16, vv = lane * 16;
    Att a;
    a.k0 = ((lane & 7) >> 1) == 0 ? ((lane & 1) ? 0xffff0000u : 0xffffu) : 0u;
    a.k1 = ((lane & 7) >> 1) == 1 ? ((lane & 1) ? 0xffff0000u : 0xffffu) : 0u;
    a.k2 = ((lane & 7) >> 1) == 2 ? ((lane & 1) ? 0xffff0000u : 0xffffu) : 0u;
    a.k3 = ((lane & 7) >> 1) == 3 ? ((lane & 1) ? 0xffff0000u : 0xffffu) : 0u;
    s.nl = wave < 2 ? 4 : 3;
    s.ldsa = L_TILES + wave * 8192;
    if (layer == 0) {
#define PARK(S) do { waitvm<16>(); tile_load<S, true>(vk, vv, rk, rv, kv0 + (4 * (S - 5) + wave) * 4096); } while (0)
      if (wave == 0) {
        waitvm<16>(); tile_load<3, true>(vk, vv, rk, rv, kv0 + T_W0 * 4096);
        waitvm<16>(); tile_load<4, true>(vk, vv, rk, rv, kv0 + (T_W0 + 1) * 4096);
      }
      for (int k = wave == 0 ? 0 : 3; k < s.nl; ++k) { waitvm<16>(); tile_dma(vk, vv, rk, rv, kv0 + (T_LDS + wave + 4 * k) * 4096, L_TILES + (wave + 4 * k) * 8192); }
      if (wave == 0) {
        PARK(5); PARK(6); PARK(7); PARK(8); PARK(9); PARK(10); PARK(11); PARK(12); PARK(13);
        { const int t = lds_claim(qctr); waitvm<16>(); ISSUE_Q(0, t); s.seq0 = s.ops; }
        { const int t = lds_claim(qctr); waitvm<16>(); ISSUE_Q(1, t); s.seq1 = s.ops; }
      } else {
        const Params* pp = kargs();
        {
          const __amdgpu_buffer_rsrc_t rg = __builtin_amdgcn_make_buffer_rsrc((void*)pp->wg, 0, D * F * 2, 0x00020000);
          const __amdgpu_buffer_rsrc_t ru = __builtin_amdgcn_make_buffer_rsrc((void*)pp->wu, 0, D * F * 2, 0x00020000);
          int vo[9];
#pragma unroll
          for (int k = 0; k < 9; ++k) { const int j = 64 * k + lane, row = (j * 7282) >> 16; vo[k] = row * (F * 2) + 4 * (j - 9 * row); }
#define GUGRP(RS, MAT, G) do { waitvm<16>(); _Pragma("unroll") for (int k = 0; k < 9; ++k) \
            dma_dword(vo[k], RS, (192 * wp + 64 * G) * (F * 2) + 36 * mu, L_IMG + ((MAT * D + 192 * wp + 64 * G) * 9 + 64 * k) * 4); } while (0)
          GUGRP(rg, 0, 0); PARK(5); GUGRP(rg, 0, 1); PARK(6); GUGRP(rg, 0, 2); PARK(7);
          GUGRP(ru, 1, 0); PARK(8); GUGRP(ru, 1, 1); PARK(9); GUGRP(ru, 1, 2); PARK(10);
#undef GUGRP
        }
        PARK(11);
        waitvm<16>();
        lds_claim(L_CTL + 12);
        PARK(12); PARK(13);
        const u64 t0 = __builtin_amdgcn_s_memrealtime();
        while (ctl[3] < 3u && spin_more<1>(t0)) {}
        asm volatile("" ::: "memory");
        LAS unsigned* img = (LAS unsigned*)(lds + L_IMG) + ((lane >> 5) * D + 18 * (lane & 31)) * 9 + 3 * wp;
        gu_repair<0>(img); gu_repair<1>(img); gu_repair<2>(img); gu_repair<3>(img); gu_repair<4>(img);
        gu_repair<5>(img); gu_repair<6>(img); gu_repair<7>(img); gu_repair<8>(img);
        lds_claim(L_CTL + 12);
        { const int t = lds_claim(qctr); waitvm<16>(); ISSUE_Q(0, t); s.seq0 = s.ops; }
        { const int t = lds_claim(qctr); waitvm<16>(); ISSUE_Q(1, t); s.seq1 = s.ops; }
        while (ctl[3] < 6u && spin_more<1>(t0)) {}
        asm volatile("" ::: "memory");
        const __amdgpu_buffer_rsrc_t ro = __builtin_amdgcn_make_buffer_rsrc((void*)pp->wo, 0, QD * D * 2, 0x00020000);
        const __amdgpu_buffer_rsrc_t rd = __builtin_amdgcn_make_buffer_rsrc((void*)pp->wd, 0, F * D * 2, 0x00020000);
#pragma unroll
        for (int i = 0; i < 6; ++i) dma1k(vv, ro, cu * (16 * D * 2) + (6 * wp + i) * 1024, L_WO + (6 * wp + i) * 1024);
#pragma unroll
        for (int i = 0; i < 7; ++i) {
          const int pc = 7 * wp + i;
          if (pc < 20 || lane < 16) dma1k(vv, rd, mu * (18 * D * 2) + pc * 1024, L_WD + pc * 1024);
        }
        s.ops += 13;
      }
#undef PARK
      { const int t = lds_claim(qctr); waitvm<16>(); ISSUE_Q(2, t); s.seq2 = s.ops; }
    } else {
      s.seq0 = s.seq1 = s.seq2 = s.ops - 64;
    }
    s.st0 = s.st1 = s.st2 = 1;
    if (wave != 0) {
      const u64 t0 = __builtin_amdgcn_s_memrealtime();
      while (ctl[2] != (unsigned)(layer + 1) && spin_more<SL0>(t0)) {}
      asm volatile("" ::: "memory");
    }
    {
      LAS v4u* q = (LAS v4u*)(lds + L_Q + (n16 >> 3) * 256 + gq * 16);
      a.q0 = q[0]; a.q1 = q[4]; a.q2 = q[8]; a.q3 = q[12];
      a.acc = f32x4{0.f, 0.f, 0.f, 0.f}; a.m = -INFINITY; a.l = 0.f;
    }
    STAMP(wave == 0 ? 3 + 12 * layer : 31);

#define STEP(S, ST, SEQ) do { if (ST != 0) { \
      int nt_ = -1; \
      if (ST == 1) nt_ = lds_claim(qctr); \
      const int yg_ = s.ops - SEQ; \
      if (yg_ >= 16) waitvm<16>(); else if (yg_ >= 8) waitvm<8>(); else waitvm<0>(); \
      for (int rep_ = 0;; ++rep_) { \
        tile_compute<S>(a); \
        if (rep_ != 0 || s.nl == 0) break; \
        tile_from_lds<S>(vv + s.ldsa); s.ldsa += 4 * 8192; --s.nl; \
      } \
      if (nt_ >= 0 && nt_ < NQ) { ISSUE_Q(S, nt_); SEQ = s.ops; } \
      else if (nt_ >= NQ && layer == 0) { \
        tile_load<S, true>(vk, vv, rk, rv, kv0 + (T_KEEP + 4 * S + wave) * 4096); s.ops += 8; SEQ = s.ops; ST = 2; } \
      else { ST = 0; } \
    } } while (0)
    int e = wave == 0 ? 0 : 2;
    for (;;) {
      STEP(0, s.st0, s.seq0);
      STEP(1, s.st1, s.seq1);
      STEP(2, s.st2, s.seq2);
      for (int r = 0; r < 2; ++r, ++e) {
        switch (e) {
          case 0: tile_compute<3>(a); break;
          case 1: tile_compute<4>(a); break;
          case 2: tile_compute<5>(a); break;
          case 3: tile_compute<6>(a); break;
          case 4: tile_compute<7>(a); break;
          case 5: tile_compute<8>(a); break;
          case 6: tile_compute<9>(a); break;
          case 7: tile_compute<10>(a); break;
          case 8: tile_compute<11>(a); break;
          case 9: tile_compute<12>(a); break;
          case 10: tile_compute<13>(a); break;
          default: break;
        }
      }
      if (e >= 11 && (s.st0 | s.st1 | s.st2) == 0) break;
    }
#undef STEP
#undef ISSUE_Q
    STAMP(wave == 0 ? 4 + 12 * layer : 27 + wave);

    {
      REFRESH();
      LAS float* mrg = (LAS float*)(lds + L_MRG) + 260 * wave;
      const float lsum = xrow_sum(a.l);
      const int h = (lane >> 3) & 1;
#pragma unroll
      for (int r = 0; r < 4; ++r) mrg[128 * h + 32 * (lane >> 4) + 8 * r + (lane & 7)] = a.acc[r];
      if ((lane & 55) == 0) { mrg[256 + 2 * h] = a.m; mrg[257 + 2 * h] = lsum; }
    }
    BAR();
    REFRESH();
    {
      LAS float* mrg = (LAS float*)(lds + L_MRG);
      const int tid = 64 * wave + lane, h = wave >> 1;
      float mw[4], M = -INFINITY;
#pragma unroll
      for (int w = 0; w < 4; ++w) { mw[w] = mrg[260 * w + 256 + 2 * h]; M = fmaxf(M, mw[w]); }
      float o = 0.f, L = 0.f;
#pragma unroll
      for (int w = 0; w < 4; ++w) { const float ew = __builtin_amdgcn_exp2f(mw[w] - M); o = fmaf(ew, mrg[260 * w + tid], o); L = fmaf(ew, mrg[260 * w + 257 + 2 * h], L); }
      __builtin_amdgcn_raw_buffer_store_b64(v2u{asu(o), tg + 1}, rw, rec + R_O + 8 * tid, 0, AUX_SC1);
      if ((tid & 127) == 0) __builtin_amdgcn_raw_buffer_store_b128(v4u{asu(M), tg + 1, asu(L), tg + 1}, rw, rec + R_ML + 16 * h, 0, AUX_SC1);
    }
    STAMP(wave == 0 ? 5 + 12 * layer : 31);
    if (wave == 0) {
      v4u g0 = {}, g1 = {}, ml = {};
      const int off = W_REC + (16 * grp + (lane >> 2)) * REC;
      const u64 t0 = __builtin_amdgcn_s_memrealtime();
      bool ok = false;
      do {
        if (!ok) {
          g0 = __builtin_amdgcn_raw_buffer_load_b128(rw, off + R_O + 128 * sp + 32 * (lane & 3), 0, AUX_SC1);
          g1 = __builtin_amdgcn_raw_buffer_load_b128(rw, off + R_O + 128 * sp + 32 * (lane & 3) + 16, 0, AUX_SC1);
          ml = __builtin_amdgcn_raw_buffer_load_b128(rw, off + R_ML + 16 * (sp >> 3), 0, AUX_SC1);
          ok = (g0[1] == tg + 1) & (g0[3] == tg + 1) & (g1[1] == tg + 1) & (g1[3] == tg + 1) & (ml[1] == tg + 1) & (ml[3] == tg + 1);
        }
      } while (!__all(ok) && spin_more<SL0>(t0));
      float M = asf(ml[0]);
      M = fmaxf(M, dpp<ROR4>(M)); M = fmaxf(M, dpp<ROR8>(M)); M = xrow_max(M);
      const float w = __builtin_amdgcn_exp2f(asf(ml[0]) - M);
      float v[5] = {w * asf(g0[0]), w * asf(g0[2]), w * asf(g1[0]), w * asf(g1[2]), w * asf(ml[2])};
#pragma unroll
      for (int k = 0; k < 5; ++k) { float t = v[k]; t += dpp<ROR4>(t); t += dpp<ROR8>(t); v[k] = xrow_sum(t); }
      const float il = __builtin_amdgcn_rcpf(v[4]);
      if (lane < 4) *(LAS v2u*)(lds + L_O + 8 * lane) = v2u{f2bf(v[0] * il) | (f2bf(v[1] * il) << 16), f2bf(v[2] * il) | (f2bf(v[3] * il) << 16)};
    }
    BAR();
    REFRESH();
    STAMP(wave == 0 ? 6 + 12 * layer : 31);
    {
      const u64 t0 = __builtin_amdgcn_s_memrealtime();
      if (wave != 0) rows_gemv_publish<2, 8>(lds, L_WO, L_O, wp, lane, rw, rec + R_Y, tg + 2);
      if (isred) {
        pause_until(t0, DLY_Y1);
        gather_y<SL_Y>(lds, rw, rho, wave, lane, tg + 2, t0);
        BAR();
        if (wave == 0) __builtin_amdgcn_raw_buffer_store_b64(v2u{finish_x(lds, lane), tg + 3}, rw, W_X + (lane >> 3) * XREP + 64 * rho + 8 * (lane & 7), 0, AUX_SC1);
        STAMP(wave == 0 ? 7 + 12 * layer : 31);
      }
      if (wave == 0) {
        REFRESH();
        v2u xp[3];
        pause_until(t0, DLY_X1);
        gather_x<SL_X>(rw, cu, lane, tg + 3, xp, t0);
        STAMP(8 + 12 * layer);
        layernorm_to_lds(xp, lds, lane);
      }
    }
    BAR();
    REFRESH();
    STAMP(wave == 0 ? 9 + 12 * layer : 31);
    if (wave != 0) {
      float acc[6];
#pragma unroll
      for (int k = 0; k < 6; ++k) acc[k] = 0.f;
      LAS unsigned* ln2 = (LAS unsigned*)(lds + L_LN) + 9 * (lane & 31);
      unsigned in2[9];
#pragma unroll
      for (int i = 0; i < 9; ++i) in2[i] = ln2[i];
      gu_pair<0>(acc, in2[0]); gu_pair<1>(acc, in2[1]); gu_pair<2>(acc, in2[2]); gu_pair<3>(acc, in2[3]); gu_pair<4>(acc, in2[4]);
      gu_pair<5>(acc, in2[5]); gu_pair<6>(acc, in2[6]); gu_pair<7>(acc, in2[7]); gu_pair<8>(acc, in2[8]);
      asm volatile("s_nop 2" : "+v"(acc[0]), "+v"(acc[1]), "+v"(acc[2]), "+v"(acc[3]), "+v"(acc[4]), "+v"(acc[5]));
      unsigned hb[3];
#pragma unroll
      for (int k = 0; k < 3; ++k) {
        float t = swap16_add(acc[k], acc[k + 3]);
        t += dpp<XOR1>(t); t += dpp<XOR2>(t); t += dpp<ROR4>(t); t += dpp<ROR8>(t);
        auto sw = __builtin_amdgcn_permlane32_swap(asu(t), asu(t), false, false);
        const float g = rbf(asf(sw[0])), u = rbf(asf(sw[1]));
        hb[k] = f2bf((g > 0.f ? g : g * 0.f) * u);
      }
      if ((lane & 47) == 0) {
        LAS u16* h = (LAS u16*)(lds + L_H) + 6 * wp + 3 * (lane >> 4);
        h[0] = (u16)hb[0]; h[1] = (u16)hb[1]; h[2] = (u16)hb[2];
      }
    }
    BAR();
    REFRESH();
    STAMP(wave == 0 ? 10 + 12 * layer : 31);
    {
      const u64 t0 = __builtin_amdgcn_s_memrealtime();
      if (wave != 0) rows_gemv_publish<3, 9>(lds, L_WD, L_H, wp, lane, rw, rec + R_Y, tg + 4);
      if (isred) {
        pause_until(t0, DLY_Y2);
        gather_y<SL_Y>(lds, rw, rho, wave, lane, tg + 4, t0);
        BAR();
        if (wave == 0) {
          const unsigned xn = finish_x(lds, lane);
          if (layer == 0) {
            __builtin_amdgcn_raw_buffer_store_b64(v2u{xn, 5u}, rw, W_X + (lane >> 3) * XREP + 64 * rho + 8 * (lane & 7), 0, AUX_SC1);
          } else {
            if (lane < 8) *(GAS unsigned*)(kargs()->out + piece_col(4 * rho + (lane >> 1)) + 2 * (lane & 1)) = xn;
            if (cu == 0 && lane == 0) { const u64 en = (((u64)ctl[5] << 32) | ctl[4]) + 2; __builtin_amdgcn_raw_buffer_store_b64(v2u{(unsigned)en, (unsigned)(en >> 32)}, rw, W_EPOCH, 0, AUX_SC1); }
          }
        }
        STAMP(wave == 0 ? 11 + 12 * layer : 31);
      }
      if (wave == 0 && layer == 0) {
        REFRESH();
        v2u xp[3];
        pause_until(t0, DLY_X2);
        gather_x<SL_X>(rw, cu, lane, 5, xp, t0);
        STAMP(12);
        layernorm_to_lds(xp, lds, lane);
      }
    }
  }
  asm volatile("" :: "a"(pin0), "a"(pin1));
}

#undef REFRESH
#undef grp
#undef sp
#undef wp
#undef mu
#undef kvb
#undef rec
#undef rho
#undef isred

}

extern "C" void kernel_launch(void* const* d_in, const int* in_sizes, int n_in, void* d_out, int out_size, void* d_ws, size_t ws_size,
                              hipStream_t stream) {
  Params p;
  p.x = (const u16*)d_in[0]; p.K = (const u16*)d_in[1]; p.V = (const u16*)d_in[2]; p.wq = (const u16*)d_in[3]; p.wo = (const u16*)d_in[4];
  p.wg = (const u16*)d_in[6]; p.wu = (const u16*)d_in[7]; p.wd = (const u16*)d_in[8];
  p.out = (u16*)d_out; p.ws = (unsigned char*)d_ws;
  (void)hipFuncSetAttribute((const void*)fused_decode, hipFuncAttributeMaxDynamicSharedMemorySize, LDS_BYTES);
  hipLaunchKernelGGL(fused_decode, dim3(NCU), dim3(256), LDS_BYTES, stream, p);
}
```

```cpp
#include <hip/hip_runtime.h>
#include <hip/hip_bf16.h>
#include <cstdint>

#define DIAG 0

namespace {

typedef unsigned v4u __attribute__((ext_vector_type(4)));
typedef unsigned v2u __attribute__((ext_vector_type(2)));
typedef float f32x4 __attribute__((ext_vector_type(4)));
typedef float f32x16 __attribute__((ext_vector_type(16)));
typedef float f32x32 __attribute__((ext_vector_type(32)));
typedef __bf16 bf16x2 __attribute__((ext_vector_type(2)));
typedef unsigned short u16;
typedef unsigned long long u64;
#define LAS __attribute__((address_space(3)))
#define GAS __attribute__((address_space(1)))

constexpr int D = 576, DH = 128, F = 4608, QD = 4096;
constexpr int NCU = 256;
constexpr float C2 = 0.088388347648318440f * 1.4426950408889634f;

constexpr int NLDS = 14;
constexpr int T_W0 = 36;
constexpr int T_LDS = 38;
constexpr int T_KEEP = T_LDS + NLDS;
constexpr int T_Q = T_KEEP + 12;
constexpr int NQ = 256 - T_Q;
#define NL2TILES 16
constexpr int NL2 = NL2TILES;
#define L2POS 3
constexpr int NPRE = L2POS;
#define ROWS3 4
constexpr int NROW3 = ROWS3, NROW4 = (NQ - 3 * NROW3) / 4;
static_assert(3 * NROW3 + 4 * NROW4 == NQ && NL2 % 4 == 0 && NQ % 4 == 0, "tile lists");

constexpr int L_TILES = 0;
constexpr int L_WO = NLDS * 8192;
constexpr int L_WD = L_WO + 16 * D * 2;
constexpr int L_MRG = L_WD + 18 * D * 2;
constexpr int L_LN = L_MRG + 4 * 260 * 4;
constexpr int L_Q = L_LN + D * 2;
constexpr int L_O = L_Q + 512;
constexpr int L_H = L_O + 64;
constexpr int L_QP = L_H + 64;
constexpr int L_CTL = L_QP + 256;
constexpr int L_END = L_CTL + 128;
constexpr int L_IMG = L_WO;
static_assert(L_IMG + 2 * D * 36 <= L_LN, "gate/up image");
constexpr int LDS_BYTES = 163840;
static_assert(L_END <= LDS_BYTES, "LDS budget");

constexpr int W_EPOCH = 0;
constexpr int NRED = 36, NREP = 8, XREP = 2560;
constexpr int W_X = 4096;
constexpr int W_Q = 32768;
constexpr int W_REC = 65536, REC = 7168;
constexpr int R_O = 0, R_Y = 2048, R_ML = R_Y + 4608;
constexpr int W_DIAG = W_REC + NCU * REC;
constexpr int W_END = W_DIAG + NCU * 64 * 8;
constexpr int AUX_SC1 = 16;

#define CODEPF 0
#define EARLYP 2
#define GUW 34
#define SL0 8
#define SL_Y 8
#define SL_X 4
#define SL_Q 4
#define DLY_Q 40
#define DLY_Y1 90
#define DLY_X1 220
#define DLY_Y2 90
#define DLY_X2 220

struct Params {
  const u16 *x, *K, *V, *wq, *wo, *wg, *wu, *wd;
  u16* out;
  unsigned char* ws;
};

__device__ __forceinline__ float rbf(float f) { return (float)(__bf16)f; }
__device__ __forceinline__ unsigned f2bf(float f) { return (unsigned)__builtin_bit_cast(u16, (__bf16)f); }
__device__ __forceinline__ float bf2f(unsigned b) { return __builtin_bit_cast(float, b << 16); }
__device__ __forceinline__ float asf(unsigned u) { return __builtin_bit_cast(float, u); }
__device__ __forceinline__ unsigned asu(float f) { return __builtin_bit_cast(unsigned, f); }
__device__ __forceinline__ float dot2(unsigned a, unsigned b, float c) {
  return __builtin_amdgcn_fdot2_f32_bf16(__builtin_bit_cast(bf16x2, a), __builtin_bit_cast(bf16x2, b), c, false);
}
template <int CTRL> __device__ __forceinline__ float dpp(float x) {
  return asf((unsigned)__builtin_amdgcn_mov_dpp((int)asu(x), CTRL, 0xf, 0xf, true));
}
constexpr int XOR1 = 0xB1, XOR2 = 0x4E, ROR4 = 0x124, ROR8 = 0x128;
__device__ __forceinline__ float swap16_add(float a, float b) {
  auto s = __builtin_amdgcn_permlane16_swap(asu(a), asu(b), false, false);
  return asf(s[0]) + asf(s[1]);
}
__device__ __forceinline__ float swap32_add(float a, float b) {
  auto s = __builtin_amdgcn_permlane32_swap(asu(a), asu(b), false, false);
  return asf(s[0]) + asf(s[1]);
}
__device__ __forceinline__ float xrow_max(float x) {
  auto s = __builtin_amdgcn_permlane16_swap(asu(x), asu(x), false, false);
  x = fmaxf(asf(s[0]), asf(s[1]));
  auto t = __builtin_amdgcn_permlane32_swap(asu(x), asu(x), false, false);
  return fmaxf(asf(t[0]), asf(t[1]));
}
__device__ __forceinline__ float xrow_sum(float x) { x = swap16_add(x, x); return swap32_add(x, x); }
__device__ __forceinline__ float wave_sum(float x) {
  x += dpp<XOR1>(x); x += dpp<XOR2>(x); x += dpp<ROR4>(x); x += dpp<ROR8>(x);
  return xrow_sum(x);
}

__device__ __forceinline__ int fresh_lane() { int l; asm volatile("v_mbcnt_lo_u32_b32 %0, -1, 0\n\tv_mbcnt_hi_u32_b32 %0, -1, %0" : "=v"(l)); return l; }
__device__ __forceinline__ unsigned zero_u() { unsigned z; asm volatile("v_mov_b32 %0, 0" : "=v"(z)); return z; }
__device__ __forceinline__ v4u zero4() { const unsigned z = zero_u(); return v4u{z, z, z, z}; }
__device__ __forceinline__ int fresh(int x) { asm volatile("" : "+s"(x)); return x; }
__device__ __forceinline__ const Params* kargs() {
  auto k = (const __attribute__((address_space(4))) Params*)__builtin_amdgcn_kernarg_segment_ptr();
  asm volatile("" : "+s"(k));
  return (const Params*)k;
}
#define BAR() asm volatile("s_waitcnt lgkmcnt(0)\n\ts_barrier" ::: "memory")
template <int N> __device__ __forceinline__ void waitvm() { asm volatile("s_waitcnt vmcnt(%c0)" :: "i"(N) : "memory"); }

template <int S> struct Slot { static constexpr bool acc = S >= 6; static constexpr int base = S >= 6 ? 32 * (S - 6) : 64 + 32 * S; };

#define LD1(P, R, VOFF, RS, OFF, POL) "buffer_load_dwordx4 " P "[%c0+" #R ":%c0+" #R "+3], " VOFF ", " RS ", %5 offen offset:" #OFF POL "\n\t"
#define SPOL " nt"
#define KPAT 2
#define KO1 64
#define KO2 128
#define KO3 192
#define XSTR(x) #x
#define LD1X(P, R, VOFF, RS, OFF, POL) LD1(P, R, VOFF, RS, OFF, POL)
#define LD8(P, POL) LD1(P, 0, "%1", "%3", 0, POL) LD1X(P, 4, "%1", "%3", KO1, POL) LD1X(P, 8, "%1", "%3", KO2, POL) LD1X(P, 12, "%1", "%3", KO3, POL) \
                    LD1(P, 16, "%2", "%4", 0, POL) LD1(P, 20, "%2", "%4", 1024, POL) LD1(P, 24, "%2", "%4", 2048, POL) LD1(P, 28, "%2", "%4", 3072, POL)
#define LD8L(P, POL) LD1(P, 0, "%1", "%3", 0, POL) LD1(P, 4, "%1", "%3", 1024, POL) LD1(P, 8, "%1", "%3", 2048, POL) LD1(P, 12, "%1", "%3", 3072, POL) \
                    LD1(P, 16, "%2", "%4", 0, POL) LD1(P, 20, "%2", "%4", 1024, POL) LD1(P, 24, "%2", "%4", 2048, POL) LD1(P, 28, "%2", "%4", 3072, POL)
template <int S, bool NT> __device__ __forceinline__ void tile_load(int vk, int vv, __amdgpu_buffer_rsrc_t rk, __amdgpu_buffer_rsrc_t rv, int soff) {
  if constexpr (Slot<S>::acc) {
    if constexpr (NT) asm volatile(LD8("a", SPOL) :: "i"(Slot<S>::base), "v"(vk), "v"(vv), "s"(rk), "s"(rv), "s"(soff) : "memory");
    else asm volatile(LD8("a", "") :: "i"(Slot<S>::base), "v"(vk), "v"(vv), "s"(rk), "s"(rv), "s"(soff) : "memory");
  } else {
    if constexpr (NT) asm volatile(LD8("v", SPOL) :: "i"(Slot<S>::base), "v"(vk), "v"(vv), "s"(rk), "s"(rv), "s"(soff) : "memory");
    else asm volatile(LD8("v", "") :: "i"(Slot<S>::base), "v"(vk), "v"(vv), "s"(rk), "s"(rv), "s"(soff) : "memory");
  }
}
__device__ __forceinline__ void dma1k(int voff, __amdgpu_buffer_rsrc_t rs, int soff, int m0v) {
  asm volatile("s_mov_b32 m0, %3\n\ts_nop 0\n\tbuffer_load_dwordx4 %0, %1, %2 offen nt lds" :: "v"(voff), "s"(rs), "s"(soff), "s"(m0v) : "memory");
}
__device__ __forceinline__ void tile_dma(int vk, int vv, __amdgpu_buffer_rsrc_t rk, __amdgpu_buffer_rsrc_t rv, int soff, int ldsb) {
#pragma unroll
  for (int i = 0; i < 4; ++i) dma1k(vk, rk, soff + KO1 * i, ldsb + 1024 * i);
#pragma unroll
  for (int i = 0; i < 4; ++i) dma1k(vv, rv, soff + 1024 * i, ldsb + 4096 + 1024 * i);
}
#define DR1(R, OFF) "ds_read_b128 v[%c0+" #R ":%c0+" #R "+3], %1 offset:" #OFF "\n\t"
template <int S> __device__ __forceinline__ void tile_from_lds(int addr) {
  static_assert(!Slot<S>::acc, "streaming slots only");
  asm volatile(DR1(0, 0) DR1(4, 1024) DR1(8, 2048) DR1(12, 3072) DR1(16, 4096) DR1(20, 5120) DR1(24, 6144) DR1(28, 7168) "s_waitcnt lgkmcnt(0)"
               :: "i"(Slot<S>::base), "v"(addr) : "memory");
}

__device__ __forceinline__ int k_lane_off(int lane) {
  return (4 * (lane & 3) + ((lane >> 2) & 3)) * 256 + (lane >> 4) * 16;
}
struct Att { v4u q0, q1, q2, q3; unsigned k0, k1, k2, k3; f32x4 acc; float m, l; };

#define QK4(P) "v_mfma_f32_16x16x32_bf16 %0, " P "[%c5:%c5+3], %1, 0\n\t" \
               "v_mfma_f32_16x16x32_bf16 %0, " P "[%c5+4:%c5+7], %2, %0\n\t" \
               "v_mfma_f32_16x16x32_bf16 %0, " P "[%c5+8:%c5+11], %3, %0\n\t" \
               "v_mfma_f32_16x16x32_bf16 %0, " P "[%c5+12:%c5+15], %4, %0\n\ts_nop 7"
#define QK4I(P) "v_mfma_f32_16x16x32_bf16 %0, " P "[%c5:%c5+3], %4, 0\n\t" \
                "v_mfma_f32_16x16x32_bf16 %1, " P "[%c5+4:%c5+7], %4, 0\n\t" \
                "v_mfma_f32_16x16x32_bf16 %2, " P "[%c5+8:%c5+11], %4, 0\n\t" \
                "v_mfma_f32_16x16x32_bf16 %3, " P "[%c5+12:%c5+15], %4, 0\n\ts_nop 7"
#define PV1(P) "s_nop 1\n\tv_mfma_f32_16x16x32_bf16 %0, " P "[%c2:%c2+3], %1, %0\n\ts_nop %c3"
template <int S> __device__ __forceinline__ void tile_compute(Att& a) {
  f32x4 s;
  if constexpr (Slot<S>::acc) asm volatile(QK4("a") : "=&v"(s) : "v"(a.q0), "v"(a.q1), "v"(a.q2), "v"(a.q3), "i"(Slot<S>::base));
  else asm volatile(QK4("v") : "=&v"(s) : "v"(a.q0), "v"(a.q1), "v"(a.q2), "v"(a.q3), "i"(Slot<S>::base));
  const float mx = xrow_max(fmaxf(fmaxf(s[0], s[1]), fmaxf(s[2], s[3])));
  const float mn = fmaxf(a.m, mx * C2);
  const float alpha = __builtin_amdgcn_exp2f(a.m - mn);
  a.m = mn;
  a.acc *= alpha;
  float ps = 0.f;
#pragma unroll
  for (int t = 0; t < 4; ++t) {
    const float p = __builtin_amdgcn_exp2f(fmaf(s[t], C2, -mn));
    ps += p;
    bf16x2 pb = {(__bf16)p, (__bf16)p};
    const unsigned pp = __builtin_bit_cast(unsigned, pb);
    const v4u b = {pp & a.k0, pp & a.k1, pp & a.k2, pp & a.k3};
    if constexpr (Slot<S>::acc) asm volatile(PV1("a") : "+v"(a.acc) : "v"(b), "i"(Slot<S>::base + 16 + 4 * t), "i"(t == 3 ? 7 : 0));
    else asm volatile(PV1("v") : "+v"(a.acc) : "v"(b), "i"(Slot<S>::base + 16 + 4 * t), "i"(t == 3 ? 7 : 0));
  }
  a.l = fmaf(a.l, alpha, ps);
}

__device__ __forceinline__ int lds_claim(int addr) {
  int t; unsigned tmp; const unsigned one = 1;
  asm volatile("s_mov_b64 exec, 1\n\tds_add_rtn_u32 %1, %2, %3\n\ts_waitcnt lgkmcnt(0)\n\tv_readfirstlane_b32 %0, %1\n\ts_mov_b64 exec, -1"
               : "=s"(t), "=&v"(tmp) : "v"(addr), "v"(one) : "memory");
  return t;
}
constexpr unsigned SEL_LO = 0x05040100u, SEL_HI = 0x07060302u;
__device__ __forceinline__ unsigned perm2(unsigned a, unsigned b, unsigned sel) { return __builtin_amdgcn_perm(b, a, sel); }

constexpr unsigned SPIN_TICKS = 20000000u;
template <int SLEEP> __device__ __forceinline__ bool spin_more(u64 t0) {
  __builtin_amdgcn_s_sleep(SLEEP);
  return (unsigned)(__builtin_amdgcn_s_memrealtime() - t0) < SPIN_TICKS;
}
__device__ __forceinline__ void pause_until(u64 t0, unsigned ticks) {
  while ((unsigned)(__builtin_amdgcn_s_memrealtime() - t0) < ticks) __builtin_amdgcn_s_sleep(1);
}

__device__ __forceinline__ int piece_col(int P) { return P < 96 ? 12 * (P >> 1) + 4 * (P & 1) : 12 * (P - 96) + 8; }

__device__ __forceinline__ void layernorm_to_lds(const v2u (&xp)[3], LAS unsigned char* lds, int lane) {
  const bool live = lane < 48;
  float x[12];
#pragma unroll
  for (int k = 0; k < 3; ++k) {
    x[4 * k] = asf(xp[k][0] << 16); x[4 * k + 1] = asf(xp[k][0] & 0xffff0000u); x[4 * k + 2] = asf(xp[k][1] << 16); x[4 * k + 3] = asf(xp[k][1] & 0xffff0000u);
  }
  float s = 0.f;
#pragma unroll
  for (int i = 0; i < 12; ++i) s += x[i];
  const float mean = wave_sum(live ? s : 0.f) * (1.f / D);
  float v = 0.f;
#pragma unroll
  for (int i = 0; i < 12; ++i) { const float d = x[i] - mean; v = fmaf(d, d, v); }
  const float var = rbf(wave_sum(live ? v : 0.f) * (1.f / D));
  const float mb = rbf(mean), inv = __builtin_amdgcn_rcpf(rbf(__builtin_amdgcn_sqrtf(rbf(var + 1e-5f))));
  if (live) {
#pragma unroll
    for (int k = 0; k < 3; ++k) {
      bf16x2 lo = {(__bf16)(rbf(x[4 * k] - mb) * inv), (__bf16)(rbf(x[4 * k + 1] - mb) * inv)};
      bf16x2 hi = {(__bf16)(rbf(x[4 * k + 2] - mb) * inv), (__bf16)(rbf(x[4 * k + 3] - mb) * inv)};
      *(LAS v2u*)(lds + L_LN + 2 * piece_col(lane + 48 * k)) = v2u{__builtin_bit_cast(unsigned, lo), __builtin_bit_cast(unsigned, hi)};
    }
  }
}

template <int NP, int NROWPAIRS>
__device__ __forceinline__ void rows_gemv_publish(LAS unsigned char* lds, int wbase, int inbase, int wp, int lane,
                                                  __amdgpu_buffer_rsrc_t rw, int ybase, unsigned tag) {
  const int rg = lane >> 4, c = lane & 15;
  float acc[12];
#pragma unroll
  for (int k = 0; k < 12; ++k) acc[k] = asf(zero_u());
#pragma unroll
  for (int i = 0; i < NP; ++i) {
    const int p = rg + 4 * i, pw = p < NROWPAIRS ? p : NROWPAIRS - 1;
    const unsigned in2 = *(LAS unsigned*)(lds + inbase + 4 * p);
    LAS v2u* r0 = (LAS v2u*)(lds + wbase + (2 * pw) * (D * 2) + 384 * wp + 24 * c);
    LAS v2u* r1 = (LAS v2u*)(lds + wbase + (2 * pw + 1) * (D * 2) + 384 * wp + 24 * c);
#pragma unroll
    for (int j = 0; j < 3; ++j) {
      const v2u a = r0[j], b = r1[j];
      acc[4 * j + 0] = dot2(perm2(a[0], b[0], SEL_LO), in2, acc[4 * j + 0]);
      acc[4 * j + 1] = dot2(perm2(a[0], b[0], SEL_HI), in2, acc[4 * j + 1]);
      acc[4 * j + 2] = dot2(perm2(a[1], b[1], SEL_LO), in2, acc[4 * j + 2]);
      acc[4 * j + 3] = dot2(perm2(a[1], b[1], SEL_HI), in2, acc[4 * j + 3]);
    }
  }
  const float y0 = swap32_add(swap16_add(acc[0], acc[2]), swap16_add(acc[4], acc[6]));
  const float y1 = swap32_add(swap16_add(acc[1], acc[3]), swap16_add(acc[5], acc[7]));
  const float y2 = swap32_add(swap16_add(acc[8], acc[9]), swap16_add(acc[10], acc[11]));
  const int idx = 64 * wp + 4 * c + rg;
  __builtin_amdgcn_raw_buffer_store_b128(v4u{asu(y0), tag, asu(y1), tag}, rw, ybase + 16 * idx, 0, AUX_SC1);
  __builtin_amdgcn_raw_buffer_store_b64(v2u{asu(y2), tag}, rw, ybase + 3072 + 8 * idx, 0, AUX_SC1);
}

template <int SLEEP>
__device__ __forceinline__ void gather_y(LAS unsigned char* lds, __amdgpu_buffer_rsrc_t rw, int rho, int wave, int lane, unsigned tag, u64 t0) {
  const int off = W_REC + (64 * wave + (lane >> 3)) * REC + R_Y + 128 * rho + 16 * (lane & 7);
  float s0, s1;
  bool ok;
  do {
    v4u a[8];
#pragma unroll
    for (int k = 0; k < 8; ++k) a[k] = __builtin_amdgcn_raw_buffer_load_b128(rw, off, k * 8 * REC, AUX_SC1);
    ok = true; s0 = 0.f; s1 = 0.f;
#pragma unroll
    for (int k = 0; k < 8; ++k) { ok &= (a[k][1] == tag) & (a[k][3] == tag); s0 += asf(a[k][0]); s1 += asf(a[k][2]); }
  } while (!__all(ok) && spin_more<SLEEP>(t0));
  s0 += dpp<ROR8>(s0); s1 += dpp<ROR8>(s1);
  s0 = xrow_sum(s0); s1 = xrow_sum(s1);
  if (lane < 8) *(LAS v2u*)(lds + L_QP + 64 * wave + 8 * lane) = v2u{asu(s0), asu(s1)};
}
__device__ __forceinline__ unsigned finish_x(LAS unsigned char* lds, int lane) {
  const int j = lane & 7;
  float s0 = 0.f, s1 = 0.f;
#pragma unroll
  for (int w = 0; w < 4; ++w) { const v2u t = *(LAS v2u*)(lds + L_QP + 64 * w + 8 * j); s0 += asf(t[0]); s1 += asf(t[1]); }
  LAS unsigned* res = (LAS unsigned*)(lds + L_CTL + 64) + j;
  const unsigned r = *res;
  bf16x2 xb = {(__bf16)(asf(r << 16) + rbf(s0)), (__bf16)(asf(r & 0xffff0000u) + rbf(s1))};
  const unsigned xn = __builtin_bit_cast(unsigned, xb);
  asm volatile("s_waitcnt lgkmcnt(0)" ::: "memory");
  *res = xn;
  return xn;
}
template <int SLEEP>
__device__ __forceinline__ void gather_x(__amdgpu_buffer_rsrc_t rw, int cu, int lane, unsigned tag, v2u (&xp)[3], u64 t0) {
  v4u g[3] = {zero4(), zero4(), zero4()};
  const int off = W_X + (cu & (NREP - 1)) * XREP + 16 * (lane < 48 ? lane : 47);
  bool ok = false;
  do {
    if (!ok) {
#pragma unroll
      for (int k = 0; k < 3; ++k) g[k] = __builtin_amdgcn_raw_buffer_load_b128(rw, off + 768 * k, 0, AUX_SC1);
      ok = (g[0][1] == tag) & (g[0][3] == tag) & (g[1][1] == tag) & (g[1][3] == tag) & (g[2][1] == tag) & (g[2][3] == tag);
    }
  } while (!__all(ok) && spin_more<SLEEP>(t0));
#pragma unroll
  for (int k = 0; k < 3; ++k) xp[k] = v2u{g[k][0], g[k][2]};
}

#define STAMP(i) do {} while (0)
#define ST1(i) do {} while (0)

template <int K> __device__ __forceinline__ void wq_load_one(int voff, __amdgpu_buffer_rsrc_t rs) {
  asm volatile("buffer_load_dwordx4 v[%c0:%c0+3], %1, %2, %3 offen" :: "i"(136 + 4 * K), "v"(voff), "s"(rs), "s"(K * QD * 2) : "memory");
}
__device__ __forceinline__ void wq_load(const u16* wq, int kap, int wp, int lane) {
  __amdgpu_buffer_rsrc_t rs = __builtin_amdgcn_make_buffer_rsrc((void*)wq, 0, D * QD * 2, 0x00020000);
  const int voff = (192 * wp + 6 * (lane >> 1)) * (QD * 2) + 32 * kap + 16 * (lane & 1);
  wq_load_one<0>(voff, rs); wq_load_one<1>(voff, rs); wq_load_one<2>(voff, rs); wq_load_one<3>(voff, rs); wq_load_one<4>(voff, rs); wq_load_one<5>(voff, rs);
}
__device__ __forceinline__ void dma_dword(int voff, __amdgpu_buffer_rsrc_t rs, int soff, int m0v) {
  asm volatile("s_mov_b32 m0, %3\n\ts_nop 0\n\tbuffer_load_dword %0, %1, %2 offen lds" :: "v"(voff), "s"(rs), "s"(soff), "s"(m0v) : "memory");
}
constexpr int wq_reg(int j) { return j < 10 ? 214 + j : 48 + (j - 10); }
template <int DST0, int DST1, int RA, int RB> __device__ __forceinline__ void pair_regs() {
  asm volatile("v_perm_b32 v%c0, v%c3, v%c2, %4\n\tv_perm_b32 v%c1, v%c3, v%c2, %5" :: "i"(DST0), "i"(DST1), "i"(RA), "i"(RB), "s"(SEL_LO), "s"(SEL_HI));
}
template <int DST> __device__ __forceinline__ void pair_vals(unsigned a, unsigned b) {
  asm volatile("v_perm_b32 v%c0, %2, %1, %3\n\tv_perm_b32 v[%c0+1], %2, %1, %4" :: "i"(DST), "v"(a), "v"(b), "s"(SEL_LO), "s"(SEL_HI));
}
template <int I> __device__ __forceinline__ void gu_repair(LAS unsigned* img) {
  const unsigned a0 = img[18 * I], a1 = img[18 * I + 1], a2 = img[18 * I + 2], b0 = img[18 * I + 9], b1 = img[18 * I + 10], b2 = img[18 * I + 11];
  pair_vals<160 + 6 * I>(a0, b0); pair_vals<162 + 6 * I>(a1, b1); pair_vals<164 + 6 * I>(a2, b2);
}
template <int I> __device__ __forceinline__ void wq_repair() {
  pair_regs<wq_reg(8 * I + 0), wq_reg(8 * I + 1), 136 + 8 * I + 0, 140 + 8 * I + 0>();
  pair_regs<wq_reg(8 * I + 2), wq_reg(8 * I + 3), 136 + 8 * I + 1, 140 + 8 * I + 1>();
  pair_regs<wq_reg(8 * I + 4), wq_reg(8 * I + 5), 136 + 8 * I + 2, 140 + 8 * I + 2>();
  pair_regs<wq_reg(8 * I + 6), wq_reg(8 * I + 7), 136 + 8 * I + 3, 140 + 8 * I + 3>();
}
template <int R0, int R1> __device__ __forceinline__ void dot_regs(float& a0, float& a1, unsigned in2) {
  asm volatile("v_dot2c_f32_bf16 %0, v%c2, %4\n\tv_dot2c_f32_bf16 %1, v%c3, %4" : "+v"(a0), "+v"(a1) : "i"(R0), "i"(R1), "v"(in2));
}
template <int I> __device__ __forceinline__ void gu_pair(float (&acc)[6], unsigned in2) {
  dot_regs<160 + 6 * I + 0, 160 + 6 * I + 1>(acc[0], acc[1], in2);
  dot_regs<160 + 6 * I + 2, 160 + 6 * I + 3>(acc[2], acc[3], in2);
  dot_regs<160 + 6 * I + 4, 160 + 6 * I + 5>(acc[4], acc[5], in2);
}
template <int I> __device__ __forceinline__ void wq_pair(float (&acc)[8], unsigned in2) {
  dot_regs<wq_reg(8 * I + 0), wq_reg(8 * I + 1)>(acc[0], acc[1], in2);
  dot_regs<wq_reg(8 * I + 2), wq_reg(8 * I + 3)>(acc[2], acc[3], in2);
  dot_regs<wq_reg(8 * I + 4), wq_reg(8 * I + 5)>(acc[4], acc[5], in2);
  dot_regs<wq_reg(8 * I + 6), wq_reg(8 * I + 7)>(acc[6], acc[7], in2);
}

struct Stream { int st0, st1, st2, seq0, seq1, seq2, ops, next, nl, ldsa; };

__global__ void __attribute__((amdgpu_num_vgpr(48))) __launch_bounds__(256, 1) fused_decode(Params p) {
  extern __shared__ __attribute__((aligned(16))) unsigned char lds_raw[];
  LAS unsigned char* lds = (LAS unsigned char*)lds_raw;
  asm volatile("" ::: "v255", "a255");
  f32x32 pin0; f32x16 pin1;
  asm volatile("" : "=a"(pin0), "=a"(pin1));
  int wave = __builtin_amdgcn_readfirstlane(threadIdx.x >> 6), cu = blockIdx.x, lane = fresh_lane();
#define REFRESH() do { lane = fresh_lane(); cu = fresh(cu); wave = fresh(wave); } while (0)
#define grp (cu >> 4)
#define sp (cu & 15)
#define wp (wave - 1)
#define mu ((cu & 7) * 32 + (cu >> 3))
#define kvb (cu << 20)
#define rec (W_REC + cu * REC)
#define rho ((cu * 9363) >> 16)
#define isred (cu == 7 * rho && cu < 7 * NRED)
  const __amdgpu_buffer_rsrc_t rk = __builtin_amdgcn_make_buffer_rsrc((void*)p.K, 0, 1 << 28, 0x00020000);
  const __amdgpu_buffer_rsrc_t rv = __builtin_amdgcn_make_buffer_rsrc((void*)p.V, 0, 1 << 28, 0x00020000);
  const __amdgpu_buffer_rsrc_t rw = __builtin_amdgcn_make_buffer_rsrc((void*)p.ws, 0, W_END, 0x00020000);
  volatile LAS unsigned* ctl = (volatile LAS unsigned*)(lds + L_CTL);
  STAMP(wave == 0 ? 0 : 31);

  if (wave == 0) {
    const v2u e = __builtin_amdgcn_raw_buffer_load_b64(rw, W_EPOCH, 0, AUX_SC1);
    v2u xp[3];
#pragma unroll
    for (int k = 0; k < 3; ++k) xp[k] = *(const GAS v2u*)(p.x + piece_col((lane < 48 ? lane : 47) + 48 * k));
    const unsigned xr = *(const GAS unsigned*)(p.x + piece_col(4 * (isred ? rho : 0) + ((lane & 7) >> 1)) + 2 * (lane & 1));
#pragma unroll
    for (int k = 0; k < 7; ++k) __builtin_amdgcn_raw_buffer_store_b128(zero4(), rw, rec + 1024 * k + 16 * lane, 0, AUX_SC1);
    if (isred) __builtin_amdgcn_raw_buffer_store_b64(v2u{0, 0}, rw, W_X + (lane >> 3) * XREP + 64 * rho + 8 * (lane & 7), 0, AUX_SC1);
    if (lane < 4) ctl[lane] = 0u;
    if (lane < 16) *(LAS unsigned*)(lds + L_H + 4 * lane) = 0u;
    if (lane < 8) ctl[16 + lane] = xr;
    if (lane == 0) { ctl[4] = e[0]; ctl[5] = e[1]; }
    layernorm_to_lds(xp, lds, lane);
  } else {
    ST1(32);
    wq_load(p.wq, mu, wp, lane);
    const int vv = lane * 16, vk = k_lane_off(lane);
#pragma unroll
    for (int k = 0; k < 3; ++k) tile_dma(vk, vv, rk, rv, kvb + (T_LDS + wave + 4 * k) * 4096, L_TILES + (wave + 4 * k) * 8192);
    ST1(33);
    if (wave == 1) { waitvm<16>(); tile_dma(vk, vv, rk, rv, kvb + (T_LDS + 13) * 4096, L_TILES + 13 * 8192); }
    waitvm<16>(); tile_load<5, true>(vk, vv, rk, rv, kvb + wave * 4096);
    waitvm<16>(); tile_load<6, true>(vk, vv, rk, rv, kvb + (4 + wave) * 4096);
    ST1(34);
  }

  Stream s;
  s.ops = 0;

  for (int layer = 0; layer < 2; ++layer) {
    const unsigned tg = 4 * layer;
    BAR();
    REFRESH();
    if (wave != 0) {
      if (layer == 0) { waitvm<16>(); wq_repair<0>(); wq_repair<1>(); wq_repair<2>(); }
      float acc[8];
#pragma unroll
      for (int k = 0; k < 8; ++k) acc[k] = asf(zero_u());
      LAS unsigned* ln2 = (LAS unsigned*)(lds + L_LN) + 96 * wp + 3 * (lane >> 1);
      const unsigned i0 = ln2[0], i1 = ln2[1], i2 = ln2[2];
      wq_pair<0>(acc, i0); wq_pair<1>(acc, i1); wq_pair<2>(acc, i2);
      asm volatile("s_nop 2" : "+v"(acc[0]), "+v"(acc[1]), "+v"(acc[2]), "+v"(acc[3]), "+v"(acc[4]), "+v"(acc[5]), "+v"(acc[6]), "+v"(acc[7]));
      float u[2];
#pragma unroll
      for (int k = 0; k < 2; ++k) {
        float t = swap16_add(swap32_add(acc[k], acc[k + 4]), swap32_add(acc[k + 2], acc[k + 6]));
        t += dpp<ROR8>(t); t += dpp<ROR4>(t); t += dpp<XOR2>(t);
        u[k] = t;
      }
      if ((lane & 14) == 0) {
        LAS float* qp = (LAS float*)(lds + L_QP) + 16 * wp + 8 * (lane & 1) + 4 * (lane >> 5) + 2 * ((lane >> 4) & 1);
        qp[0] = u[0]; qp[1] = u[1];
      }
    }
    BAR();
    REFRESH();
    STAMP(wave == 0 ? 1 + 12 * layer : 31);
    const int nstream = layer == 0 ? (wave == 0 ? NROW4 : NROW4 + NROW3) : NQ / 4;
    auto qtile = [&](int i) -> int {
      if (layer == 0) return T_Q + (wave == 0 ? 3 * NROW3 + 4 * i : (i < NROW3 ? 3 * i + wave - 1 : 4 * i - NROW3 + wave));
      return T_Q + wave + 4 * (i < NPRE ? i : (i < NPRE + NL2 / 4 ? NQ / 4 - NL2 / 4 + (i - NPRE) : i - NL2 / 4));
    };
#define ISSUE_Q(S) do { const int id_ = qtile(s.next); const int so_ = kv0 + id_ * 4096; ++s.next; \
      if (layer == 0 && id_ >= 256 - NL2) tile_load<S, false>(vk, vv, rk, rv, so_); else tile_load<S, true>(vk, vv, rk, rv, so_); s.ops += 8; } while (0)
    if (wave == 0) {
      const u64 epoch = ((u64)(unsigned)__builtin_amdgcn_readfirstlane(ctl[5]) << 32) | (unsigned)__builtin_amdgcn_readfirstlane(ctl[4]);
      const u64 tag = (epoch + 1 + layer) << 16;
      if (lane < 16) {
        LAS float* qp = (LAS float*)(lds + L_QP) + lane;
        const u64 g = tag | f2bf((qp[0] + qp[16]) + qp[32]);
        if (layer == 0) waitvm<0>();
        __builtin_amdgcn_raw_buffer_store_b64(v2u{(unsigned)g, (unsigned)(g >> 32)}, rw, W_Q + mu * 128 + 8 * lane, 0, AUX_SC1);
      }
      const unsigned tlo = (unsigned)tag >> 16, thi = (unsigned)(tag >> 32);
      {
        v4u g0 = zero4(), g1 = zero4();
        const u64 t0 = __builtin_amdgcn_s_memrealtime();
        bool ok = false;
        pause_until(t0, DLY_Q);
        do {
          if (!ok) {
            g0 = __builtin_amdgcn_raw_buffer_load_b128(rw, W_Q + 2048 * grp + 32 * lane, 0, AUX_SC1);
            g1 = __builtin_amdgcn_raw_buffer_load_b128(rw, W_Q + 2048 * grp + 32 * lane + 16, 0, AUX_SC1);
            ok = ((g0[0] >> 16) == tlo) & (g0[1] == thi) & ((g0[2] >> 16) == tlo) & (g0[3] == thi) &
                 ((g1[0] >> 16) == tlo) & (g1[1] == thi) & ((g1[2] >> 16) == tlo) & (g1[3] == thi);
          }
        } while (!__all(ok) && spin_more<SL_Q>(t0));
        *(LAS v2u*)(lds + L_Q + 8 * lane) = v2u{perm2(g0[0], g0[2], SEL_LO), perm2(g1[0], g1[2], SEL_LO)};
        asm volatile("s_waitcnt lgkmcnt(0)" ::: "memory");
        ctl[2] = layer + 1;
        STAMP(layer == 0 ? 24 : 31);
      }
      if (layer == 0) {
        const u64 t0 = __builtin_amdgcn_s_memrealtime();
        bool ok = false;
        do {
          if (!ok) {
            ok = true;
#pragma unroll
            for (int k = 0; k < 4; ++k) {
              const v2u g = __builtin_amdgcn_raw_buffer_load_b64(rw, W_Q + 128 * (lane + 64 * k), 0, AUX_SC1);
              ok &= ((g[0] >> 16) == tlo) & (g[1] == thi);
            }
          }
        } while (!__all(ok) && spin_more<SL0>(t0));
      }
    }
    STAMP(wave == 0 ? 2 + 12 * layer : 31);
    REFRESH();
    const int n16 = lane & 15, gq = lane >> 4, kv0 = kvb;
    const int vk = k_lane_off(lane), vv = lane * 16;
    Att a;
    a.k0 = ((lane & 7) >> 1) == 0 ? ((lane & 1) ? 0xffff0000u : 0xffffu) : 0u;
    a.k1 = ((lane & 7) >> 1) == 1 ? ((lane & 1) ? 0xffff0000u : 0xffffu) : 0u;
    a.k2 = ((lane & 7) >> 1) == 2 ? ((lane & 1) ? 0xffff0000u : 0xffffu) : 0u;
    a.k3 = ((lane & 7) >> 1) == 3 ? ((lane & 1) ? 0xffff0000u : 0xffffu) : 0u;
    s.nl = wave < 2 ? 4 : 3;
    s.ldsa = L_TILES + wave * 8192;
    s.next = 0;
    if (layer == 0) {
#define PARK(S) do { waitvm<16>(); tile_load<S, true>(vk, vv, rk, rv, kv0 + (4 * (S - 5) + wave) * 4096); } while (0)
      if (wave == 0) {
        waitvm<16>(); tile_load<3, true>(vk, vv, rk, rv, kv0 + T_W0 * 4096);
        waitvm<16>(); tile_load<4, true>(vk, vv, rk, rv, kv0 + (T_W0 + 1) * 4096);
      }
      if (wave == 0) {
        for (int k = 0; k < 4; ++k) { waitvm<16>(); tile_dma(vk, vv, rk, rv, kv0 + (T_LDS + 4 * k) * 4096, L_TILES + 4 * k * 8192); }
        PARK(5); PARK(6); PARK(7); PARK(8); PARK(9); PARK(10); PARK(11); PARK(12); PARK(13);
        waitvm<16>(); ISSUE_Q(0); s.seq0 = s.ops;
        waitvm<16>(); ISSUE_Q(1); s.seq1 = s.ops;
        waitvm<16>(); ISSUE_Q(2); s.seq2 = s.ops;
      } else {
        const Params* pp = kargs();
        {
          const __amdgpu_buffer_rsrc_t rg = __builtin_amdgcn_make_buffer_rsrc((void*)pp->wg, 0, D * F * 2, 0x00020000);
          const __amdgpu_buffer_rsrc_t ru = __builtin_amdgcn_make_buffer_rsrc((void*)pp->wu, 0, D * F * 2, 0x00020000);
          int vo[9];
#pragma unroll
          for (int k = 0; k < 9; ++k) { const int j = 64 * k + lane, row = (j * 7282) >> 16; vo[k] = row * (F * 2) + 4 * (j - 9 * row); }
#define GUGRP(RS, MAT, G) do { _Pragma("unroll") for (int k = 0; k < 9; ++k) \
            dma_dword(vo[k], RS, (192 * wp + 64 * G) * (F * 2) + 36 * mu, L_IMG + ((MAT * D + 192 * wp + 64 * G) * 9 + 64 * k) * 4); } while (0)
#define PARKD(S) do { waitvm<GUW>(); tile_load<S, true>(vk, vv, rk, rv, kv0 + (4 * (S - 5) + wave) * 4096); } while (0)
          ST1(35);
          PARK(7);
          GUGRP(rg, 0, 0); PARKD(8); GUGRP(rg, 0, 1); PARKD(9); GUGRP(rg, 0, 2); PARKD(10);
          ST1(36);
          GUGRP(ru, 1, 0); PARKD(11); GUGRP(ru, 1, 1); PARKD(12); GUGRP(ru, 1, 2); PARKD(13);
#undef PARKD
#undef GUGRP
        }
        ST1(37);
        waitvm<GUW - 9>(); ISSUE_Q(0); s.seq0 = s.ops;
        waitvm<16>();
        lds_claim(L_CTL + 12);
        ISSUE_Q(1); s.seq1 = s.ops;
        const u64 t0 = __builtin_amdgcn_s_memrealtime();
        while (ctl[3] < 3u && spin_more<1>(t0)) {}
        asm volatile("" ::: "memory");
        ST1(38);
        LAS unsigned* img = (LAS unsigned*)(lds + L_IMG) + ((lane >> 5) * D + 18 * (lane & 31)) * 9 + 3 * wp;
        gu_repair<0>(img); gu_repair<1>(img); gu_repair<2>(img); gu_repair<3>(img); gu_repair<4>(img);
        gu_repair<5>(img); gu_repair<6>(img); gu_repair<7>(img); gu_repair<8>(img);
        lds_claim(L_CTL + 12);
        waitvm<16>(); ISSUE_Q(2); s.seq2 = s.ops;
        ST1(39);
        while (ctl[3] < 6u && spin_more<1>(t0)) {}
        asm volatile("" ::: "memory");
        const __amdgpu_buffer_rsrc_t ro = __builtin_amdgcn_make_buffer_rsrc((void*)pp->wo, 0, QD * D * 2, 0x00020000);
        const __amdgpu_buffer_rsrc_t rd = __builtin_amdgcn_make_buffer_rsrc((void*)pp->wd, 0, F * D * 2, 0x00020000);
#pragma unroll
        for (int i = 0; i < 6; ++i) dma1k(vv, ro, cu * (16 * D * 2) + (6 * wp + i) * 1024, L_WO + (6 * wp + i) * 1024);
#pragma unroll
        for (int i = 0; i < 7; ++i) {
          const int pc = 7 * wp + i;
          if (pc < 20 || lane < 16) dma1k(vv, rd, mu * (18 * D * 2) + pc * 1024, L_WD + pc * 1024);
        }
        s.ops += 13;
      }
#undef PARK
    } else {
      s.seq0 = s.seq1 = s.seq2 = s.ops - 64;
    }
    s.st0 = s.st1 = s.st2 = 1;
    if (wave != 0) {
      const u64 t0 = __builtin_amdgcn_s_memrealtime();
      while (ctl[2] != (unsigned)(layer + 1) && spin_more<1>(t0)) {}
      asm volatile("" ::: "memory");
    }
    {
      LAS v4u* q = (LAS v4u*)(lds + L_Q + (n16 >> 3) * 256 + gq * 16);
      a.q0 = q[0]; a.q1 = q[4]; a.q2 = q[8]; a.q3 = q[12];
      a.acc = __builtin_bit_cast(f32x4, zero4()); a.m = -INFINITY; a.l = asf(zero_u());
    }
    STAMP(wave == 0 ? 3 + 12 * layer : (wave == 1 && layer == 0 ? 40 : 31));

#define STEP(S, ST, SEQ) do { if (ST != 0) { \
      const int yg_ = s.ops - SEQ; \
      if (yg_ >= 16) waitvm<16>(); else if (yg_ >= 8) waitvm<8>(); else waitvm<0>(); \
      for (int rep_ = 0;; ++rep_) { \
        tile_compute<S>(a); \
        if (rep_ != 0 || s.nl == 0) break; \
        tile_from_lds<S>(vv + s.ldsa); s.ldsa += 4 * 8192; --s.nl; \
      } \
      if (s.next < nstream) { PACE(); ISSUE_Q(S); SEQ = s.ops; PROG(); } \
      else if (ST == 1 && layer == 0) { \
        tile_load<S, true>(vk, vv, rk, rv, kv0 + (T_KEEP + 4 * S + wave) * 4096); s.ops += 8; SEQ = s.ops; ST = 2; } \
      else { ST = 0; } \
    } } while (0)
#define PACE() do {} while (0)
#define PROG() do {} while (0)
    int e = wave == 0 ? 0 : 2;
    for (;;) {
      STEP(0, s.st0, s.seq0);
      STEP(1, s.st1, s.seq1);
      STEP(2, s.st2, s.seq2);
      for (int r = 0; r < 2; ++r, ++e) {
        switch (e) {
          case 0: tile_compute<3>(a); break;
          case 1: tile_compute<4>(a); break;
          case 2: tile_compute<5>(a); break;
          case 3: tile_compute<6>(a); break;
          case 4: tile_compute<7>(a); break;
          case 5: tile_compute<8>(a); break;
          case 6: tile_compute<9>(a); break;
          case 7: tile_compute<10>(a); break;
          case 8: tile_compute<11>(a); break;
          case 9: tile_compute<12>(a); break;
          case 10: tile_compute<13>(a); break;
          default: break;
        }
      }
      if (e >= 11 && (s.st0 | s.st1 | s.st2) == 0) break;
    }
#undef STEP
#undef PROG
#undef PACE
#undef ISSUE_Q
    STAMP(wave == 0 ? 4 + 12 * layer : 27 + wave);

    {
      REFRESH();
      LAS float* mrg = (LAS float*)(lds + L_MRG) + 260 * wave;
      const float lsum = xrow_sum(a.l);
      const int h = (lane >> 3) & 1;
#pragma unroll
      for (int r = 0; r < 4; ++r) mrg[128 * h + 32 * (lane >> 4) + 8 * r + (lane & 7)] = a.acc[r];
      if ((lane & 55) == 0) { mrg[256 + 2 * h] = a.m; mrg[257 + 2 * h] = lsum; }
    }
    BAR();
    REFRESH();
    {
      LAS float* mrg = (LAS float*)(lds + L_MRG);
      const int tid = 64 * wave + lane, h = wave >> 1;
      float mw[4], M = -INFINITY;
#pragma unroll
      for (int w = 0; w < 4; ++w) { mw[w] = mrg[260 * w + 256 + 2 * h]; M = fmaxf(M, mw[w]); }
      float o = 0.f, L = 0.f;
#pragma unroll
      for (int w = 0; w < 4; ++w) { const float ew = __builtin_amdgcn_exp2f(mw[w] - M); o = fmaf(ew, mrg[260 * w + tid], o); L = fmaf(ew, mrg[260 * w + 257 + 2 * h], L); }
      __builtin_amdgcn_raw_buffer_store_b64(v2u{asu(o), tg + 1}, rw, rec + R_O + 8 * tid, 0, AUX_SC1);
      if ((tid & 127) == 0) __builtin_amdgcn_raw_buffer_store_b128(v4u{asu(M), tg + 1, asu(L), tg + 1}, rw, rec + R_ML + 16 * h, 0, AUX_SC1);
    }
    STAMP(wave == 0 ? 5 + 12 * layer : 31);
    if (wave == 0) {
      v4u g0 = zero4(), g1 = zero4(), ml = zero4();
      const int off = W_REC + (16 * grp + (lane >> 2)) * REC;
      const u64 t0 = __builtin_amdgcn_s_memrealtime();
      bool ok = false;
      do {
        if (!ok) {
          g0 = __builtin_amdgcn_raw_buffer_load_b128(rw, off + R_O + 128 * sp + 32 * (lane & 3), 0, AUX_SC1);
          g1 = __builtin_amdgcn_raw_buffer_load_b128(rw, off + R_O + 128 * sp + 32 * (lane & 3) + 16, 0, AUX_SC1);
          ml = __builtin_amdgcn_raw_buffer_load_b128(rw, off + R_ML + 16 * (sp >> 3), 0, AUX_SC1);
          ok = (g0[1] == tg + 1) & (g0[3] == tg + 1) & (g1[1] == tg + 1) & (g1[3] == tg + 1) & (ml[1] == tg + 1) & (ml[3] == tg + 1);
        }
      } while (!__all(ok) && spin_more<SL0>(t0));
      float M = asf(ml[0]);
      M = fmaxf(M, dpp<ROR4>(M)); M = fmaxf(M, dpp<ROR8>(M)); M = xrow_max(M);
      const float w = __builtin_amdgcn_exp2f(asf(ml[0]) - M);
      float v[5] = {w * asf(g0[0]), w * asf(g0[2]), w * asf(g1[0]), w * asf(g1[2]), w * asf(ml[2])};
#pragma unroll
      for (int k = 0; k < 5; ++k) { float t = v[k]; t += dpp<ROR4>(t); t += dpp<ROR8>(t); v[k] = xrow_sum(t); }
      const float il = __builtin_amdgcn_rcpf(v[4]);
      if (lane < 4) *(LAS v2u*)(lds + L_O + 8 * lane) = v2u{f2bf(v[0] * il) | (f2bf(v[1] * il) << 16), f2bf(v[2] * il) | (f2bf(v[3] * il) << 16)};
    }
    BAR();
    REFRESH();
    STAMP(wave == 0 ? 6 + 12 * layer : 31);
    {
      const u64 t0 = __builtin_amdgcn_s_memrealtime();
      if (wave != 0) rows_gemv_publish<2, 8>(lds, L_WO, L_O, wp, lane, rw, rec + R_Y, tg + 2);
      if (isred) {
        pause_until(t0, DLY_Y1);
        gather_y<SL_Y>(lds, rw, rho, wave, lane, tg + 2, t0);
        BAR();
        if (wave == 0) __builtin_amdgcn_raw_buffer_store_b64(v2u{finish_x(lds, lane), tg + 3}, rw, W_X + (lane >> 3) * XREP + 64 * rho + 8 * (lane & 7), 0, AUX_SC1);
        STAMP(wave == 0 ? 7 + 12 * layer : 31);
      }
      if (wave == 0) {
        REFRESH();
        v2u xp[3];
        pause_until(t0, DLY_X1);
        gather_x<SL_X>(rw, cu, lane, tg + 3, xp, t0);
        STAMP(8 + 12 * layer);
        layernorm_to_lds(xp, lds, lane);
      }
    }
    BAR();
    REFRESH();
    STAMP(wave == 0 ? 9 + 12 * layer : 31);
    if (wave != 0) {
      float acc[6];
#pragma unroll
      for (int k = 0; k < 6; ++k) acc[k] = asf(zero_u());
      LAS unsigned* ln2 = (LAS unsigned*)(lds + L_LN) + 9 * (lane & 31);
      unsigned in2[9];
#pragma unroll
      for (int i = 0; i < 9; ++i) in2[i] = ln2[i];
      gu_pair<0>(acc, in2[0]); gu_pair<1>(acc, in2[1]); gu_pair<2>(acc, in2[2]); gu_pair<3>(acc, in2[3]); gu_pair<4>(acc, in2[4]);
      gu_pair<5>(acc, in2[5]); gu_pair<6>(acc, in2[6]); gu_pair<7>(acc, in2[7]); gu_pair<8>(acc, in2[8]);
      asm volatile("s_nop 2" : "+v"(acc[0]), "+v"(acc[1]), "+v"(acc[2]), "+v"(acc[3]), "+v"(acc[4]), "+v"(acc[5]));
      unsigned hb[3];
#pragma unroll
      for (int k = 0; k < 3; ++k) {
        float t = swap16_add(acc[k], acc[k + 3]);
        t += dpp<XOR1>(t); t += dpp<XOR2>(t); t += dpp<ROR4>(t); t += dpp<ROR8>(t);
        auto sw = __builtin_amdgcn_permlane32_swap(asu(t), asu(t), false, false);
        const float g = rbf(asf(sw[0])), u = rbf(asf(sw[1]));
        hb[k] = f2bf((g > 0.f ? g : g * 0.f) * u);
      }
      if ((lane & 47) == 0) {
        LAS u16* h = (LAS u16*)(lds + L_H) + 6 * wp + 3 * (lane >> 4);
        h[0] = (u16)hb[0]; h[1] = (u16)hb[1]; h[2] = (u16)hb[2];
      }
    }
    BAR();
    REFRESH();
    STAMP(wave == 0 ? 10 + 12 * layer : 31);
    {
      const u64 t0 = __builtin_amdgcn_s_memrealtime();
      if (wave != 0) rows_gemv_publish<3, 9>(lds, L_WD, L_H, wp, lane, rw, rec + R_Y, tg + 4);
      if (isred) {
        pause_until(t0, DLY_Y2);
        gather_y<SL_Y>(lds, rw, rho, wave, lane, tg + 4, t0);
        BAR();
        if (wave == 0) {
          const unsigned xn = finish_x(lds, lane);
          if (layer == 0) {
            __builtin_amdgcn_raw_buffer_store_b64(v2u{xn, 5u}, rw, W_X + (lane >> 3) * XREP + 64 * rho + 8 * (lane & 7), 0, AUX_SC1);
          } else {
            const __amdgpu_buffer_rsrc_t ro = __builtin_amdgcn_make_buffer_rsrc((void*)kargs()->out, 0, D * 2, 0x00020000);
            if (lane < 8) __builtin_amdgcn_raw_buffer_store_b32(xn, ro, 2 * piece_col(4 * rho + (lane >> 1)) + 4 * (lane & 1), 0, 0);
            if (cu == 0 && lane == 0) { const u64 en = (((u64)ctl[5] << 32) | ctl[4]) + 2; __builtin_amdgcn_raw_buffer_store_b64(v2u{(unsigned)en, (unsigned)(en >> 32)}, rw, W_EPOCH, 0, AUX_SC1); }
          }
        }
        STAMP(wave == 0 ? 11 + 12 * layer : 31);
      }
      if (wave == 0 && layer == 0) {
        REFRESH();
        v2u xp[3];
        pause_until(t0, DLY_X2);
        gather_x<SL_X>(rw, cu, lane, 5, xp, t0);
        STAMP(12);
        layernorm_to_lds(xp, lds, lane);
      }
    }
  }
  asm volatile("" :: "a"(pin0), "a"(pin1));
}

#undef REFRESH
#undef grp
#undef sp
#undef wp
#undef mu
#undef kvb
#undef rec
#undef rho
#undef isred

}

extern "C" void kernel_launch(void* const* d_in, const int* in_sizes, int n_in, void* d_out, int out_size, void* d_ws, size_t ws_size,
                              hipStream_t stream) {
  Params p;
  p.x = (const u16*)d_in[0]; p.K = (const u16*)d_in[1]; p.V = (const u16*)d_in[2]; p.wq = (const u16*)d_in[3]; p.wo = (const u16*)d_in[4];
  p.wg = (const u16*)d_in[6]; p.wu = (const u16*)d_in[7]; p.wd = (const u16*)d_in[8];
  p.out = (u16*)d_out; p.ws = (unsigned char*)d_ws;
  (void)hipFuncSetAttribute((const void*)fused_decode, hipFuncAttributeMaxDynamicSharedMemorySize, LDS_BYTES);
  hipLaunchKernelGGL(fused_decode, dim3(NCU), dim3(256), LDS_BYTES, stream, p);
}
```

```cpp
#include <hip/hip_runtime.h>
#include <hip/hip_bf16.h>
#include <cstdint>

#define DIAG 0

namespace {

typedef unsigned v4u __attribute__((ext_vector_type(4)));
typedef unsigned v2u __attribute__((ext_vector_type(2)));
typedef float f32x4 __attribute__((ext_vector_type(4)));
typedef float f32x16 __attribute__((ext_vector_type(16)));
typedef float f32x32 __attribute__((ext_vector_type(32)));
typedef __bf16 bf16x2 __attribute__((ext_vector_type(2)));
typedef unsigned short u16;
typedef unsigned long long u64;
#define LAS __attribute__((address_space(3)))
#define GAS __attribute__((address_space(1)))

constexpr int D = 576, DH = 128, F = 4608, QD = 4096;
constexpr int NCU = 256;
constexpr float C2 = 0.088388347648318440f * 1.4426950408889634f;

constexpr int NLDS = 14;
constexpr int T_W0 = 36;
constexpr int T_LDS = 38;
constexpr int T_KEEP = T_LDS + NLDS;
constexpr int T_Q = T_KEEP + 12;
constexpr int NQ = 256 - T_Q;
#define NL2TILES 16
constexpr int NL2 = NL2TILES;
#define L2POS 3
constexpr int NPRE = L2POS;
#define ROWS3 4
constexpr int NROW3 = ROWS3, NROW4 = (NQ - 3 * NROW3) / 4;
static_assert(3 * NROW3 + 4 * NROW4 == NQ && NL2 % 4 == 0 && NQ % 4 == 0, "tile lists");

constexpr int L_TILES = 0;
constexpr int L_WO = NLDS * 8192;
constexpr int L_WD = L_WO + 16 * D * 2;
constexpr int L_MRG = L_WD + 18 * D * 2;
constexpr int L_LN = L_MRG + 4 * 260 * 4;
constexpr int L_Q = L_LN + D * 2;
constexpr int L_O = L_Q + 512;
constexpr int L_H = L_O + 64;
constexpr int L_QP = L_H + 64;
constexpr int L_CTL = L_QP + 256;
constexpr int L_END = L_CTL + 128;
constexpr int L_IMG = L_WO;
static_assert(L_IMG + 2 * D * 36 <= L_LN, "gate/up image");
constexpr int LDS_BYTES = 163840;
static_assert(L_END <= LDS_BYTES, "LDS budget");

constexpr int W_EPOCH = 0;
constexpr int NRED = 36, NREP = 8, XREP = 2560;
constexpr int W_X = 4096;
constexpr int W_FLAG = 28672;
constexpr int W_Q = 32768;
constexpr int W_REC = 65536, REC = 7168;
constexpr int R_O = 0, R_Y = 2048, R_ML = R_Y + 4608;
constexpr int W_DIAG = W_REC + NCU * REC;
constexpr int W_END = W_DIAG + NCU * 64 * 8;
constexpr int AUX_SC1 = 16;

#define LDSLAP 8
#define LDSALL 0
#define XPL 1
#define PTHR 16
#define LIN_L2 true
#define L2PER 3
constexpr int L2MUL = 256 / L2PER + 1;
#define LIN_KEEP false
#define LIN_HEAD 0
#define LEVEL_PAR 0
#define LEVEL_STR 1
constexpr unsigned STRADDLE = 0x11224488u;
#define EARLYP 2
#define GUW 34
#define SL0 8
#define SL_Y 8
#define SL_X 4
#define SL_Q 4
#define DLY_Q 40
#define DLY_Y1 90
#define DLY_X1 220
#define DLY_Y2 90
#define DLY_X2 220

struct Params {
  const u16 *x, *K, *V, *wq, *wo, *wg, *wu, *wd;
  u16* out;
  unsigned char* ws;
};

__device__ __forceinline__ float rbf(float f) { return (float)(__bf16)f; }
__device__ __forceinline__ unsigned f2bf(float f) { return (unsigned)__builtin_bit_cast(u16, (__bf16)f); }
__device__ __forceinline__ float bf2f(unsigned b) { return __builtin_bit_cast(float, b << 16); }
__device__ __forceinline__ float asf(unsigned u) { return __builtin_bit_cast(float, u); }
__device__ __forceinline__ unsigned asu(float f) { return __builtin_bit_cast(unsigned, f); }
__device__ __forceinline__ float dot2(unsigned a, unsigned b, float c) {
  return __builtin_amdgcn_fdot2_f32_bf16(__builtin_bit_cast(bf16x2, a), __builtin_bit_cast(bf16x2, b), c, false);
}
template <int CTRL> __device__ __forceinline__ float dpp(float x) {
  return asf((unsigned)__builtin_amdgcn_mov_dpp((int)asu(x), CTRL, 0xf, 0xf, true));
}
constexpr int XOR1 = 0xB1, XOR2 = 0x4E, ROR4 = 0x124, ROR8 = 0x128;
__device__ __forceinline__ float swap16_add(float a, float b) {
  auto s = __builtin_amdgcn_permlane16_swap(asu(a), asu(b), false, false);
  return asf(s[0]) + asf(s[1]);
}
__device__ __forceinline__ float swap32_add(float a, float b) {
  auto s = __builtin_amdgcn_permlane32_swap(asu(a), asu(b), false, false);
  return asf(s[0]) + asf(s[1]);
}
__device__ __forceinline__ float xrow_max(float x) {
  auto s = __builtin_amdgcn_permlane16_swap(asu(x), asu(x), false, false);
  x = fmaxf(asf(s[0]), asf(s[1]));
  auto t = __builtin_amdgcn_permlane32_swap(asu(x), asu(x), false, false);
  return fmaxf(asf(t[0]), asf(t[1]));
}
__device__ __forceinline__ float xrow_sum(float x) { x = swap16_add(x, x); return swap32_add(x, x); }
__device__ __forceinline__ float wave_sum(float x) {
  x += dpp<XOR1>(x); x += dpp<XOR2>(x); x += dpp<ROR4>(x); x += dpp<ROR8>(x);
  return xrow_sum(x);
}

__device__ __forceinline__ int fresh_lane() { int l; asm volatile("v_mbcnt_lo_u32_b32 %0, -1, 0\n\tv_mbcnt_hi_u32_b32 %0, -1, %0" : "=v"(l)); return l; }
__device__ __forceinline__ unsigned zero_u() { unsigned z; asm volatile("v_mov_b32 %0, 0" : "=v"(z)); return z; }
__device__ __forceinline__ v4u zero4() { const unsigned z = zero_u(); return v4u{z, z, z, z}; }
__device__ __forceinline__ int fresh(int x) { asm volatile("" : "+s"(x)); return x; }
__device__ __forceinline__ const Params* kargs() {
  auto k = (const __attribute__((address_space(4))) Params*)__builtin_amdgcn_kernarg_segment_ptr();
  asm volatile("" : "+s"(k));
  return (const Params*)k;
}
#define BAR() asm volatile("s_waitcnt lgkmcnt(0)\n\ts_barrier" ::: "memory")
template <int N> __device__ __forceinline__ void waitvm() { asm volatile("s_waitcnt vmcnt(%c0)" :: "i"(N) : "memory"); }

template <int S> struct Slot { static constexpr bool acc = S >= 6; static constexpr int base = S >= 6 ? 32 * (S - 6) : 64 + 32 * S; };

#define LD1(P, R, VOFF, RS, OFF, POL) "buffer_load_dwordx4 " P "[%c0+" #R ":%c0+" #R "+3], " VOFF ", " RS ", %5 offen offset:" #OFF POL "\n\t"
#define LDV(P, POL) LD1(P, 16, "%2", "%4", 0, POL) LD1(P, 20, "%2", "%4", 1024, POL) LD1(P, 24, "%2", "%4", 2048, POL) LD1(P, 28, "%2", "%4", 3072, POL)
#define LD8R(P, POL) LD1(P, 0, "%1", "%3", 0, POL) LD1(P, 4, "%1", "%3", 64, POL) LD1(P, 8, "%1", "%3", 128, POL) LD1(P, 12, "%1", "%3", 192, POL) LDV(P, POL)
#define LD8L(P, POL) LD1(P, 0, "%1", "%3", 0, POL) LD1(P, 4, "%1", "%3", 1024, POL) LD1(P, 8, "%1", "%3", 2048, POL) LD1(P, 12, "%1", "%3", 3072, POL) LDV(P, POL)
#define LDOPS :: "i"(Slot<S>::base), "v"(vk), "v"(vv), "s"(rk), "s"(rv), "s"(soff) : "memory"
template <int S, bool NT, bool LIN>
__device__ __forceinline__ void tile_load(int vk, int vv, __amdgpu_buffer_rsrc_t rk, __amdgpu_buffer_rsrc_t rv, int soff) {
  if constexpr (Slot<S>::acc) {
    static_assert(NT && LIN, "resident slots");
    asm volatile(LD8L("a", " nt") LDOPS);
  } else if constexpr (LIN) {
    if constexpr (NT) asm volatile(LD8L("v", " nt") LDOPS); else asm volatile(LD8L("v", "") LDOPS);
  } else {
    if constexpr (NT) asm volatile(LD8R("v", " nt") LDOPS); else asm volatile(LD8R("v", "") LDOPS);
  }
}
__device__ __forceinline__ int k_off_rows(int lane) { return (4 * (lane & 3) + ((lane >> 2) & 3)) * 256 + (lane >> 4) * 16; }
__device__ __forceinline__ int k_off_lin(int lane) { return ((lane >> 2) & 3) * 256 + (lane >> 4) * 64 + (lane & 3) * 16; }
__device__ __forceinline__ void dma1k(int voff, __amdgpu_buffer_rsrc_t rs, int soff, int m0v) {
  asm volatile("s_mov_b32 m0, %3\n\ts_nop 0\n\tbuffer_load_dwordx4 %0, %1, %2 offen nt lds" :: "v"(voff), "s"(rs), "s"(soff), "s"(m0v) : "memory");
}
__device__ __forceinline__ void tile_dma(int vk, int vv, __amdgpu_buffer_rsrc_t rk, __amdgpu_buffer_rsrc_t rv, int soff, int ldsb) {
#pragma unroll
  for (int i = 0; i < 4; ++i) dma1k(vk, rk, soff + 1024 * i, ldsb + 1024 * i);
#pragma unroll
  for (int i = 0; i < 4; ++i) dma1k(vv, rv, soff + 1024 * i, ldsb + 4096 + 1024 * i);
}
#define DR1(R, OFF) "ds_read_b128 v[%c0+" #R ":%c0+" #R "+3], %1 offset:" #OFF "\n\t"
template <int S> __device__ __forceinline__ void tile_from_lds(int addr) {
  static_assert(!Slot<S>::acc, "streaming slots only");
  asm volatile(DR1(0, 0) DR1(4, 1024) DR1(8, 2048) DR1(12, 3072) DR1(16, 4096) DR1(20, 5120) DR1(24, 6144) DR1(28, 7168) "s_waitcnt lgkmcnt(0)"
               :: "i"(Slot<S>::base), "v"(addr) : "memory");
}

struct Att { unsigned k0, k1, k2, k3; f32x4 acc; float m, l; };
struct QFrag { v4u q0, q1, q2, q3; };
template <bool LIN> __device__ __forceinline__ QFrag q_frag(int addr) {
  QFrag q;
  LAS v4u* p = (LAS v4u*)(uintptr_t)(unsigned)addr;
  q.q0 = p[0];
  if constexpr (!LIN) { q.q1 = p[4]; q.q2 = p[8]; q.q3 = p[12]; }
  return q;
}

#define QK4(P) "v_mfma_f32_16x16x32_bf16 %0, " P "[%c5:%c5+3], %1, 0\n\t" \
               "v_mfma_f32_16x16x32_bf16 %0, " P "[%c5+4:%c5+7], %2, %0\n\t" \
               "v_mfma_f32_16x16x32_bf16 %0, " P "[%c5+8:%c5+11], %3, %0\n\t" \
               "v_mfma_f32_16x16x32_bf16 %0, " P "[%c5+12:%c5+15], %4, %0\n\ts_nop 7"
#define QK4I(P) "v_mfma_f32_16x16x32_bf16 %0, " P "[%c5:%c5+3], %4, 0\n\t" \
                "v_mfma_f32_16x16x32_bf16 %1, " P "[%c5+4:%c5+7], %4, 0\n\t" \
                "v_mfma_f32_16x16x32_bf16 %2, " P "[%c5+8:%c5+11], %4, 0\n\t" \
                "v_mfma_f32_16x16x32_bf16 %3, " P "[%c5+12:%c5+15], %4, 0\n\ts_nop 7"
#define PV1(P) "s_nop 1\n\tv_mfma_f32_16x16x32_bf16 %0, " P "[%c2:%c2+3], %1, %0\n\ts_nop %c3"
template <int S, bool LIN> __device__ __forceinline__ void tile_compute(Att& a, const QFrag& q) {
  f32x4 s;
  if constexpr (LIN) {
    f32x4 d0, d1, d2, d3;
    if constexpr (Slot<S>::acc) asm volatile(QK4I("a") : "=&v"(d0), "=&v"(d1), "=&v"(d2), "=&v"(d3) : "v"(q.q0), "i"(Slot<S>::base));
    else asm volatile(QK4I("v") : "=&v"(d0), "=&v"(d1), "=&v"(d2), "=&v"(d3) : "v"(q.q0), "i"(Slot<S>::base));
    s[0] = (dpp<0x00>(d0[0]) + dpp<0x55>(d0[1])) + (dpp<0xAA>(d0[2]) + dpp<0xFF>(d0[3]));
    s[1] = (dpp<0x00>(d1[0]) + dpp<0x55>(d1[1])) + (dpp<0xAA>(d1[2]) + dpp<0xFF>(d1[3]));
    s[2] = (dpp<0x00>(d2[0]) + dpp<0x55>(d2[1])) + (dpp<0xAA>(d2[2]) + dpp<0xFF>(d2[3]));
    s[3] = (dpp<0x00>(d3[0]) + dpp<0x55>(d3[1])) + (dpp<0xAA>(d3[2]) + dpp<0xFF>(d3[3]));
  } else {
    static_assert(LIN || !Slot<S>::acc, "resident slots are linear");
    asm volatile(QK4("v") : "=&v"(s) : "v"(q.q0), "v"(q.q1), "v"(q.q2), "v"(q.q3), "i"(Slot<S>::base));
  }
  const float mx = xrow_max(fmaxf(fmaxf(s[0], s[1]), fmaxf(s[2], s[3])));
  const float mn = fmaxf(a.m, mx * C2);
  const float alpha = __builtin_amdgcn_exp2f(a.m - mn);
  a.m = mn;
  a.acc *= alpha;
  float ps = 0.f;
#pragma unroll
  for (int t = 0; t < 4; ++t) {
    const float p = __builtin_amdgcn_exp2f(fmaf(s[t], C2, -mn));
    ps += p;
    bf16x2 pb = {(__bf16)p, (__bf16)p};
    const unsigned pp = __builtin_bit_cast(unsigned, pb);
    const v4u b = {pp & a.k0, pp & a.k1, pp & a.k2, pp & a.k3};
    if constexpr (Slot<S>::acc) asm volatile(PV1("a") : "+v"(a.acc) : "v"(b), "i"(Slot<S>::base + 16 + 4 * t), "i"(t == 3 ? 7 : 0));
    else asm volatile(PV1("v") : "+v"(a.acc) : "v"(b), "i"(Slot<S>::base + 16 + 4 * t), "i"(t == 3 ? 7 : 0));
  }
  a.l = fmaf(a.l, alpha, ps);
}

__device__ __forceinline__ int lds_claim(int addr) {
  int t; unsigned tmp; const unsigned one = 1;
  asm volatile("s_mov_b64 exec, 1\n\tds_add_rtn_u32 %1, %2, %3\n\ts_waitcnt lgkmcnt(0)\n\tv_readfirstlane_b32 %0, %1\n\ts_mov_b64 exec, -1"
               : "=s"(t), "=&v"(tmp) : "v"(addr), "v"(one) : "memory");
  return t;
}
constexpr unsigned SEL_LO = 0x05040100u, SEL_HI = 0x07060302u;
__device__ __forceinline__ unsigned perm2(unsigned a, unsigned b, unsigned sel) { return __builtin_amdgcn_perm(b, a, sel); }

constexpr unsigned SPIN_TICKS = 20000000u;
template <int SLEEP> __device__ __forceinline__ bool spin_more(u64 t0) {
  __builtin_amdgcn_s_sleep(SLEEP);
  return (unsigned)(__builtin_amdgcn_s_memrealtime() - t0) < SPIN_TICKS;
}
__device__ __forceinline__ void pause_until(u64 t0, unsigned ticks) {
  while ((unsigned)(__builtin_amdgcn_s_memrealtime() - t0) < ticks) __builtin_amdgcn_s_sleep(1);
}

__device__ __forceinline__ int piece_col(int P) { return P < 96 ? 12 * (P >> 1) + 4 * (P & 1) : 12 * (P - 96) + 8; }

__device__ __forceinline__ void layernorm_to_lds(const v2u (&xp)[3], LAS unsigned char* lds, int lane) {
  const bool live = lane < 48;
  float x[12];
#pragma unroll
  for (int k = 0; k < 3; ++k) {
    x[4 * k] = asf(xp[k][0] << 16); x[4 * k + 1] = asf(xp[k][0] & 0xffff0000u); x[4 * k + 2] = asf(xp[k][1] << 16); x[4 * k + 3] = asf(xp[k][1] & 0xffff0000u);
  }
  float s = 0.f;
#pragma unroll
  for (int i = 0; i < 12; ++i) s += x[i];
  const float mean = wave_sum(live ? s : 0.f) * (1.f / D);
  float v = 0.f;
#pragma unroll
  for (int i = 0; i < 12; ++i) { const float d = x[i] - mean; v = fmaf(d, d, v); }
  const float var = rbf(wave_sum(live ? v : 0.f) * (1.f / D));
  const float mb = rbf(mean), inv = __builtin_amdgcn_rcpf(rbf(__builtin_amdgcn_sqrtf(rbf(var + 1e-5f))));
  if (live) {
#pragma unroll
    for (int k = 0; k < 3; ++k) {
      bf16x2 lo = {(__bf16)(rbf(x[4 * k] - mb) * inv), (__bf16)(rbf(x[4 * k + 1] - mb) * inv)};
      bf16x2 hi = {(__bf16)(rbf(x[4 * k + 2] - mb) * inv), (__bf16)(rbf(x[4 * k + 3] - mb) * inv)};
      *(LAS v2u*)(lds + L_LN + 2 * piece_col(lane + 48 * k)) = v2u{__builtin_bit_cast(unsigned, lo), __builtin_bit_cast(unsigned, hi)};
    }
  }
}

template <int NP, int NROWPAIRS>
__device__ __forceinline__ void rows_gemv_publish(LAS unsigned char* lds, int wbase, int inbase, int wp, int lane,
                                                  __amdgpu_buffer_rsrc_t rw, int ybase, unsigned tag) {
  const int rg = lane >> 4, c = lane & 15;
  float acc[12];
#pragma unroll
  for (int k = 0; k < 12; ++k) acc[k] = asf(zero_u());
#pragma unroll
  for (int i = 0; i < NP; ++i) {
    const int p = rg + 4 * i, pw = p < NROWPAIRS ? p : NROWPAIRS - 1;
    const unsigned in2 = *(LAS unsigned*)(lds + inbase + 4 * p);
    LAS v2u* r0 = (LAS v2u*)(lds + wbase + (2 * pw) * (D * 2) + 384 * wp + 24 * c);
    LAS v2u* r1 = (LAS v2u*)(lds + wbase + (2 * pw + 1) * (D * 2) + 384 * wp + 24 * c);
#pragma unroll
    for (int j = 0; j < 3; ++j) {
      const v2u a = r0[j], b = r1[j];
      acc[4 * j + 0] = dot2(perm2(a[0], b[0], SEL_LO), in2, acc[4 * j + 0]);
      acc[4 * j + 1] = dot2(perm2(a[0], b[0], SEL_HI), in2, acc[4 * j + 1]);
      acc[4 * j + 2] = dot2(perm2(a[1], b[1], SEL_LO), in2, acc[4 * j + 2]);
      acc[4 * j + 3] = dot2(perm2(a[1], b[1], SEL_HI), in2, acc[4 * j + 3]);
    }
  }
  const float y0 = swap32_add(swap16_add(acc[0], acc[2]), swap16_add(acc[4], acc[6]));
  const float y1 = swap32_add(swap16_add(acc[1], acc[3]), swap16_add(acc[5], acc[7]));
  const float y2 = swap32_add(swap16_add(acc[8], acc[9]), swap16_add(acc[10], acc[11]));
  const int idx = 64 * wp + 4 * c + rg;
  __builtin_amdgcn_raw_buffer_store_b128(v4u{asu(y0), tag, asu(y1), tag}, rw, ybase + 16 * idx, 0, AUX_SC1);
  __builtin_amdgcn_raw_buffer_store_b64(v2u{asu(y2), tag}, rw, ybase + 3072 + 8 * idx, 0, AUX_SC1);
}

template <int SLEEP>
__device__ __forceinline__ void gather_y(LAS unsigned char* lds, __amdgpu_buffer_rsrc_t rw, int rho, int wave, int lane, unsigned tag, u64 t0) {
  const int off = W_REC + (64 * wave + (lane >> 3)) * REC + R_Y + 128 * rho + 16 * (lane & 7);
  float s0, s1;
  bool ok;
  do {
    v4u a[8];
#pragma unroll
    for (int k = 0; k < 8; ++k) a[k] = __builtin_amdgcn_raw_buffer_load_b128(rw, off, k * 8 * REC, AUX_SC1);
    ok = true; s0 = 0.f; s1 = 0.f;
#pragma unroll
    for (int k = 0; k < 8; ++k) { ok &= (a[k][1] == tag) & (a[k][3] == tag); s0 += asf(a[k][0]); s1 += asf(a[k][2]); }
  } while (!__all(ok) && spin_more<SLEEP>(t0));
  s0 += dpp<ROR8>(s0); s1 += dpp<ROR8>(s1);
  s0 = xrow_sum(s0); s1 = xrow_sum(s1);
  if (lane < 8) *(LAS v2u*)(lds + L_QP + 64 * wave + 8 * lane) = v2u{asu(s0), asu(s1)};
}
__device__ __forceinline__ unsigned finish_x(LAS unsigned char* lds, int lane) {
  const int j = lane & 7;
  float s0 = 0.f, s1 = 0.f;
#pragma unroll
  for (int w = 0; w < 4; ++w) { const v2u t = *(LAS v2u*)(lds + L_QP + 64 * w + 8 * j); s0 += asf(t[0]); s1 += asf(t[1]); }
  LAS unsigned* res = (LAS unsigned*)(lds + L_CTL + 64) + j;
  const unsigned r = *res;
  bf16x2 xb = {(__bf16)(asf(r << 16) + rbf(s0)), (__bf16)(asf(r & 0xffff0000u) + rbf(s1))};
  const unsigned xn = __builtin_bit_cast(unsigned, xb);
  asm volatile("s_waitcnt lgkmcnt(0)" ::: "memory");
  *res = xn;
  return xn;
}
template <int SLEEP>
__device__ __forceinline__ void gather_x(__amdgpu_buffer_rsrc_t rw, int cu, int lane, unsigned tag, v2u (&xp)[3], u64 t0) {
  v4u g[3] = {zero4(), zero4(), zero4()};
  const int off = W_X + (cu & (NREP - 1)) * XREP + 16 * (lane < 48 ? lane : 47);
  bool ok = false;
  do {
    if (!ok) {
#pragma unroll
      for (int k = 0; k < 3; ++k) g[k] = __builtin_amdgcn_raw_buffer_load_b128(rw, off + 768 * k, 0, AUX_SC1);
      ok = (g[0][1] == tag) & (g[0][3] == tag) & (g[1][1] == tag) & (g[1][3] == tag) & (g[2][1] == tag) & (g[2][3] == tag);
    }
  } while (!__all(ok) && spin_more<SLEEP>(t0));
#pragma unroll
  for (int k = 0; k < 3; ++k) xp[k] = v2u{g[k][0], g[k][2]};
}

#define STAMP(i) do {} while (0)
#define ST1(i) do {} while (0)

template <int K> __device__ __forceinline__ void wq_load_one(int voff, __amdgpu_buffer_rsrc_t rs) {
  asm volatile("buffer_load_dwordx4 v[%c0:%c0+3], %1, %2, %3 offen" :: "i"(136 + 4 * K), "v"(voff), "s"(rs), "s"(K * QD * 2) : "memory");
}
__device__ __forceinline__ void wq_load(const u16* wq, int kap, int wp, int lane) {
  __amdgpu_buffer_rsrc_t rs = __builtin_amdgcn_make_buffer_rsrc((void*)wq, 0, D * QD * 2, 0x00020000);
  const int voff = (192 * wp + 6 * (lane >> 1)) * (QD * 2) + 32 * kap + 16 * (lane & 1);
  wq_load_one<0>(voff, rs); wq_load_one<1>(voff, rs); wq_load_one<2>(voff, rs); wq_load_one<3>(voff, rs); wq_load_one<4>(voff, rs); wq_load_one<5>(voff, rs);
}
__device__ __forceinline__ void dma_dword(int voff, __amdgpu_buffer_rsrc_t rs, int soff, int m0v) {
  asm volatile("s_mov_b32 m0, %3\n\ts_nop 0\n\tbuffer_load_dword %0, %1, %2 offen lds" :: "v"(voff), "s"(rs), "s"(soff), "s"(m0v) : "memory");
}
constexpr int wq_reg(int j) { return j < 10 ? 214 + j : 48 + (j - 10); }
template <int DST0, int DST1, int RA, int RB> __device__ __forceinline__ void pair_regs() {
  asm volatile("v_perm_b32 v%c0, v%c3, v%c2, %4\n\tv_perm_b32 v%c1, v%c3, v%c2, %5" :: "i"(DST0), "i"(DST1), "i"(RA), "i"(RB), "s"(SEL_LO), "s"(SEL_HI));
}
template <int DST> __device__ __forceinline__ void pair_vals(unsigned a, unsigned b) {
  asm volatile("v_perm_b32 v%c0, %2, %1, %3\n\tv_perm_b32 v[%c0+1], %2, %1, %4" :: "i"(DST), "v"(a), "v"(b), "s"(SEL_LO), "s"(SEL_HI));
}
template <int I> __device__ __forceinline__ void gu_repair(LAS unsigned* img) {
  const unsigned a0 = img[18 * I], a1 = img[18 * I + 1], a2 = img[18 * I + 2], b0 = img[18 * I + 9], b1 = img[18 * I + 10], b2 = img[18 * I + 11];
  pair_vals<160 + 6 * I>(a0, b0); pair_vals<162 + 6 * I>(a1, b1); pair_vals<164 + 6 * I>(a2, b2);
}
template <int I> __device__ __forceinline__ void wq_repair() {
  pair_regs<wq_reg(8 * I + 0), wq_reg(8 * I + 1), 136 + 8 * I + 0, 140 + 8 * I + 0>();
  pair_regs<wq_reg(8 * I + 2), wq_reg(8 * I + 3), 136 + 8 * I + 1, 140 + 8 * I + 1>();
  pair_regs<wq_reg(8 * I + 4), wq_reg(8 * I + 5), 136 + 8 * I + 2, 140 + 8 * I + 2>();
  pair_regs<wq_reg(8 * I + 6), wq_reg(8 * I + 7), 136 + 8 * I + 3, 140 + 8 * I + 3>();
}
template <int R0, int R1> __device__ __forceinline__ void dot_regs(float& a0, float& a1, unsigned in2) {
  asm volatile("v_dot2c_f32_bf16 %0, v%c2, %4\n\tv_dot2c_f32_bf16 %1, v%c3, %4" : "+v"(a0), "+v"(a1) : "i"(R0), "i"(R1), "v"(in2));
}
template <int I> __device__ __forceinline__ void gu_pair(float (&acc)[6], unsigned in2) {
  dot_regs<160 + 6 * I + 0, 160 + 6 * I + 1>(acc[0], acc[1], in2);
  dot_regs<160 + 6 * I + 2, 160 + 6 * I + 3>(acc[2], acc[3], in2);
  dot_regs<160 + 6 * I + 4, 160 + 6 * I + 5>(acc[4], acc[5], in2);
}
template <int I> __device__ __forceinline__ void wq_pair(float (&acc)[8], unsigned in2) {
  dot_regs<wq_reg(8 * I + 0), wq_reg(8 * I + 1)>(acc[0], acc[1], in2);
  dot_regs<wq_reg(8 * I + 2), wq_reg(8 * I + 3)>(acc[2], acc[3], in2);
  dot_regs<wq_reg(8 * I + 4), wq_reg(8 * I + 5)>(acc[4], acc[5], in2);
  dot_regs<wq_reg(8 * I + 6), wq_reg(8 * I + 7)>(acc[6], acc[7], in2);
}

struct Stream { int st0, st1, st2, seq0, seq1, seq2, ops, next, nl, ldsa; };

__global__ void __attribute__((amdgpu_num_vgpr(48))) __launch_bounds__(256, 1) fused_decode(Params p) {
  extern __shared__ __attribute__((aligned(16))) unsigned char lds_raw[];
  LAS unsigned char* lds = (LAS unsigned char*)lds_raw;
  asm volatile("" ::: "v255", "a255");
  f32x32 pin0; f32x16 pin1;
  asm volatile("" : "=a"(pin0), "=a"(pin1));
  int wave = __builtin_amdgcn_readfirstlane(threadIdx.x >> 6), cu = blockIdx.x, lane = fresh_lane();
#define REFRESH() do { lane = fresh_lane(); cu = fresh(cu); wave = fresh(wave); } while (0)
#define grp (cu >> 4)
#define sp (cu & 15)
#define wp (wave - 1)
#define mu ((cu & 7) * 32 + (cu >> 3))
#define kvb (cu << 20)
#define rec (W_REC + cu * REC)
#define rho ((cu * 9363) >> 16)
#define isred (cu == 7 * rho && cu < 7 * NRED)
  const __amdgpu_buffer_rsrc_t rk = __builtin_amdgcn_make_buffer_rsrc((void*)p.K, 0, 1 << 28, 0x00020000);
  const __amdgpu_buffer_rsrc_t rv = __builtin_amdgcn_make_buffer_rsrc((void*)p.V, 0, 1 << 28, 0x00020000);
  const __amdgpu_buffer_rsrc_t rw = __builtin_amdgcn_make_buffer_rsrc((void*)p.ws, 0, W_END, 0x00020000);
  volatile LAS unsigned* ctl = (volatile LAS unsigned*)(lds + L_CTL);
  STAMP(wave == 0 ? 0 : 31);

  if (wave == 0) {
    const v2u e = __builtin_amdgcn_raw_buffer_load_b64(rw, W_EPOCH, 0, AUX_SC1);
    v2u xp[3];
#pragma unroll
    for (int k = 0; k < 3; ++k) xp[k] = *(const GAS v2u*)(p.x + piece_col((lane < 48 ? lane : 47) + 48 * k));
    const unsigned xr = *(const GAS unsigned*)(p.x + piece_col(4 * (isred ? rho : 0) + ((lane & 7) >> 1)) + 2 * (lane & 1));
#pragma unroll
    for (int k = 0; k < 7; ++k) __builtin_amdgcn_raw_buffer_store_b128(zero4(), rw, rec + 1024 * k + 16 * lane, 0, AUX_SC1);
    if (isred) __builtin_amdgcn_raw_buffer_store_b64(v2u{0, 0}, rw, W_X + (lane >> 3) * XREP + 64 * rho + 8 * (lane & 7), 0, AUX_SC1);
    if (lane < 4) ctl[lane] = 0u;
    if (lane < 16) *(LAS unsigned*)(lds + L_H + 4 * lane) = 0u;
    if (lane < 8) ctl[16 + lane] = xr;
    if (lane == 0) { ctl[4] = e[0]; ctl[5] = e[1]; }
    layernorm_to_lds(xp, lds, lane);
    waitvm<0>();
    const u64 tag = ((((u64)e[1] << 32) | e[0]) + 1) << 16;
    if (lane == 0) __builtin_amdgcn_raw_buffer_store_b64(v2u{(unsigned)tag, (unsigned)(tag >> 32)}, rw, W_FLAG + 8 * cu, 0, AUX_SC1);
  } else {
    ST1(32);
    const int vv = lane * 16, vk = k_off_lin(lane);
    wq_load(p.wq, mu, wp, lane);
#pragma unroll
    for (int k = 0; k < 3; ++k) tile_dma(vk, vv, rk, rv, kvb + (T_LDS + wave + 4 * k) * 4096, L_TILES + (wave + 4 * k) * 8192);
    ST1(33);
    if (wave == 1) { waitvm<PTHR>(); tile_dma(vk, vv, rk, rv, kvb + (T_LDS + 13) * 4096, L_TILES + 13 * 8192); }
    waitvm<PTHR>(); tile_load<5, true, true>(vk, vv, rk, rv, kvb + wave * 4096);
    waitvm<PTHR>(); tile_load<6, true, true>(vk, vv, rk, rv, kvb + (4 + wave) * 4096);
    ST1(34);
  }

  Stream s;
  s.ops = 0;

  for (int layer = 0; layer < 2; ++layer) {
    const unsigned tg = 4 * layer;
    BAR();
    REFRESH();
    if (wave != 0) {
      if (layer == 0) { waitvm<16>(); wq_repair<0>(); wq_repair<1>(); wq_repair<2>(); }
      float acc[8];
#pragma unroll
      for (int k = 0; k < 8; ++k) acc[k] = asf(zero_u());
      LAS unsigned* ln2 = (LAS unsigned*)(lds + L_LN) + 96 * wp + 3 * (lane >> 1);
      const unsigned i0 = ln2[0], i1 = ln2[1], i2 = ln2[2];
      wq_pair<0>(acc, i0); wq_pair<1>(acc, i1); wq_pair<2>(acc, i2);
      asm volatile("s_nop 2" : "+v"(acc[0]), "+v"(acc[1]), "+v"(acc[2]), "+v"(acc[3]), "+v"(acc[4]), "+v"(acc[5]), "+v"(acc[6]), "+v"(acc[7]));
      float u[2];
#pragma unroll
      for (int k = 0; k < 2; ++k) {
        float t = swap16_add(swap32_add(acc[k], acc[k + 4]), swap32_add(acc[k + 2], acc[k + 6]));
        t += dpp<ROR8>(t); t += dpp<ROR4>(t); t += dpp<XOR2>(t);
        u[k] = t;
      }
      if ((lane & 14) == 0) {
        LAS float* qp = (LAS float*)(lds + L_QP) + 16 * wp + 8 * (lane & 1) + 4 * (lane >> 5) + 2 * ((lane >> 4) & 1);
        qp[0] = u[0]; qp[1] = u[1];
      }
    }
    BAR();
    REFRESH();
    STAMP(wave == 0 ? 1 + 12 * layer : 31);
    if (wave == 0) {
      const u64 epoch = ((u64)(unsigned)__builtin_amdgcn_readfirstlane(ctl[5]) << 32) | (unsigned)__builtin_amdgcn_readfirstlane(ctl[4]);
      const u64 tag = (epoch + 1 + layer) << 16;
      if (lane < 16) {
        LAS float* qp = (LAS float*)(lds + L_QP) + lane;
        const u64 g = tag | f2bf((qp[0] + qp[16]) + qp[32]);
        __builtin_amdgcn_raw_buffer_store_b64(v2u{(unsigned)g, (unsigned)(g >> 32)}, rw, W_Q + mu * 128 + 8 * lane, 0, AUX_SC1);
      }
      const unsigned tlo = (unsigned)tag >> 16, thi = (unsigned)(tag >> 32);
      {
        v4u g0 = zero4(), g1 = zero4();
        const u64 t0 = __builtin_amdgcn_s_memrealtime();
        bool ok = false;
        pause_until(t0, DLY_Q);
        do {
          if (!ok) {
            g0 = __builtin_amdgcn_raw_buffer_load_b128(rw, W_Q + 2048 * grp + 32 * lane, 0, AUX_SC1);
            g1 = __builtin_amdgcn_raw_buffer_load_b128(rw, W_Q + 2048 * grp + 32 * lane + 16, 0, AUX_SC1);
            ok = ((g0[0] >> 16) == tlo) & (g0[1] == thi) & ((g0[2] >> 16) == tlo) & (g0[3] == thi) &
                 ((g1[0] >> 16) == tlo) & (g1[1] == thi) & ((g1[2] >> 16) == tlo) & (g1[3] == thi);
          }
        } while (!__all(ok) && spin_more<SL_Q>(t0));
        *(LAS v2u*)(lds + L_Q + 8 * lane) = v2u{perm2(g0[0], g0[2], SEL_LO), perm2(g1[0], g1[2], SEL_LO)};
        asm volatile("s_waitcnt lgkmcnt(0)" ::: "memory");
        ctl[2] = layer + 1;
        STAMP(layer == 0 ? 24 : 31);
      }
      if (layer == 0) {
        const u64 t0 = __builtin_amdgcn_s_memrealtime();
        bool ok;
        do {
          const v4u f0 = __builtin_amdgcn_raw_buffer_load_b128(rw, W_FLAG + 16 * lane, 0, AUX_SC1);
          const v4u f1 = __builtin_amdgcn_raw_buffer_load_b128(rw, W_FLAG + 1024 + 16 * lane, 0, AUX_SC1);
          ok = (f0[0] == tlo << 16) & (f0[1] == thi) & (f0[2] == tlo << 16) & (f0[3] == thi) &
               (f1[0] == tlo << 16) & (f1[1] == thi) & (f1[2] == tlo << 16) & (f1[3] == thi);
        } while (!__all(ok) && spin_more<32>(t0));
      }
    }
    STAMP(wave == 0 ? 2 + 12 * layer : 31);
    REFRESH();
    const int kv0 = kvb;
    const int vk = k_off_lin(lane), vkr = k_off_rows(lane), vv = lane * 16;
    const int qal = L_Q + (lane & 8) * 32 + (lane >> 4) * 64 + (lane & 3) * 16, qar = L_Q + (lane & 8) * 32 + (lane >> 4) * 16;
    int x0, x1, x2, x3, nx = 0;
    {
      const int r0 = (T_Q + (layer == 0 ? 3 * NROW3 : 0) + wave) * 4096, r1 = r0 + 4 * 4096;
      const bool par = LEVEL_PAR && layer == 1 && wave < 2, str = LEVEL_STR && layer == 0 && wave != 0;
      const bool sgive = (STRADDLE >> (cu >> 3)) & 1, stake = (STRADDLE >> ((cu >> 3) ^ 1)) & 1;
      x0 = x1 = x2 = x3 = 0;
      auto push = [&](int so) { x3 = x2; x2 = x1; x1 = x0; x0 = so; ++nx; };
      if (!(par && (cu & 1) == 0)) push(kv0 + r0);
      if (par && (cu & 1) == 1) push(((cu ^ 1) << 20) + r0);
      if (!(str && sgive)) push(kv0 + r1);
      if (str && stake) push(((cu ^ 8) << 20) + r1);
    }
    const int nstream = nx - 2 + (layer == 0 ? (wave == 0 ? NROW4 : NROW4 + NROW3) : NQ / 4);
    auto next_off = [&](int n) -> int {
      if (n < nx) return n == 0 ? x0 : n == 1 ? x1 : n == 2 ? x2 : x3;
      const int i = n - nx;
      if (layer == 0) return kv0 + 4096 * (T_Q + (wave == 0 ? 3 * NROW3 + 8 + 4 * i : (i < NROW3 ? 3 * i + wave - 1 : 4 * i - NROW3 + 8 + wave)));
      const int k = i - NPRE, t = (k * L2MUL) >> 8, r = k - L2PER * t;
      const int row = k < 0 ? i + 2 : (k >= L2PER * (NL2 / 4) ? i + 2 - NL2 / 4 : (r == L2PER - 1 ? NQ / 4 - NL2 / 4 + t : i + 2 - t));
      return kv0 + 4096 * (T_Q + wave + 4 * row);
    };
#define ISSUE_Q(S, ST, SEQ) do { const int so_ = next_off(s.next); \
      const bool lin_ = layer == 0 ? s.next < LIN_HEAD : (LIN_L2 && (unsigned)(s.next - nx - NPRE) < (unsigned)(L2PER * (NL2 / 4))); \
      if (layer == 0 && (so_ & 0xfffff) >= (256 - NL2) * 4096) tile_load<S, false, false>(vkr, vv, rk, rv, so_); \
      else if (lin_) tile_load<S, true, true>(vk, vv, rk, rv, so_); \
      else tile_load<S, true, false>(vkr, vv, rk, rv, so_); \
      ST = lin_ ? 5 : 1; \
      ++s.next; s.ops += 8; SEQ = s.ops; } while (0)
    Att a;
    a.k0 = ((lane & 7) >> 1) == 0 ? ((lane & 1) ? 0xffff0000u : 0xffffu) : 0u;
    a.k1 = ((lane & 7) >> 1) == 1 ? ((lane & 1) ? 0xffff0000u : 0xffffu) : 0u;
    a.k2 = ((lane & 7) >> 1) == 2 ? ((lane & 1) ? 0xffff0000u : 0xffffu) : 0u;
    a.k3 = ((lane & 7) >> 1) == 3 ? ((lane & 1) ? 0xffff0000u : 0xffffu) : 0u;
    s.nl = wave < 2 ? 4 : 3;
    s.ldsa = L_TILES + wave * 8192;
    s.next = 0;
    if (layer == 0) {
#define PARK(S) do { waitvm<PTHR>(); tile_load<S, true, true>(vk, vv, rk, rv, kv0 + (4 * (S - 5) + wave) * 4096); } while (0)
      if (wave == 0) {
        waitvm<PTHR>(); tile_load<3, true, true>(vk, vv, rk, rv, kv0 + T_W0 * 4096);
        waitvm<PTHR>(); tile_load<4, true, true>(vk, vv, rk, rv, kv0 + (T_W0 + 1) * 4096);
      }
      if (wave == 0) {
        for (int k = 0; k < 4; ++k) { waitvm<PTHR>(); tile_dma(vk, vv, rk, rv, kv0 + (T_LDS + 4 * k) * 4096, L_TILES + 4 * k * 8192); }
        PARK(5); PARK(6); PARK(7); PARK(8); PARK(9); PARK(10); PARK(11); PARK(12); PARK(13);
        waitvm<PTHR>(); ISSUE_Q(0, s.st0, s.seq0);
        waitvm<PTHR>(); ISSUE_Q(1, s.st1, s.seq1);
        waitvm<PTHR>(); ISSUE_Q(2, s.st2, s.seq2);
      } else {
        const Params* pp = kargs();
        {
          const __amdgpu_buffer_rsrc_t rg = __builtin_amdgcn_make_buffer_rsrc((void*)pp->wg, 0, D * F * 2, 0x00020000);
          const __amdgpu_buffer_rsrc_t ru = __builtin_amdgcn_make_buffer_rsrc((void*)pp->wu, 0, D * F * 2, 0x00020000);
          int vo[9];
#pragma unroll
          for (int k = 0; k < 9; ++k) { const int j = 64 * k + lane, row = (j * 7282) >> 16; vo[k] = row * (F * 2) + 4 * (j - 9 * row); }
#define GUGRP(RS, MAT, G) do { _Pragma("unroll") for (int k = 0; k < 9; ++k) \
            dma_dword(vo[k], RS, (192 * wp + 64 * G) * (F * 2) + 36 * mu, L_IMG + ((MAT * D + 192 * wp + 64 * G) * 9 + 64 * k) * 4); } while (0)
#define PARKD(S) do { waitvm<GUW>(); tile_load<S, true, true>(vk, vv, rk, rv, kv0 + (4 * (S - 5) + wave) * 4096); } while (0)
          ST1(35);
          PARK(7);
          GUGRP(rg, 0, 0); PARKD(8); GUGRP(rg, 0, 1); PARKD(9); GUGRP(rg, 0, 2); PARKD(10);
          ST1(36);
          GUGRP(ru, 1, 0); PARKD(11); GUGRP(ru, 1, 1); PARKD(12); GUGRP(ru, 1, 2); PARKD(13);
#undef PARKD
#undef GUGRP
        }
        ST1(37);
        waitvm<GUW - 9>(); ISSUE_Q(0, s.st0, s.seq0);
        waitvm<16>();
        lds_claim(L_CTL + 12);
        ISSUE_Q(1, s.st1, s.seq1);
        const u64 t0 = __builtin_amdgcn_s_memrealtime();
        while (ctl[3] < 3u && spin_more<1>(t0)) {}
        asm volatile("" ::: "memory");
        ST1(38);
        LAS unsigned* img = (LAS unsigned*)(lds + L_IMG) + ((lane >> 5) * D + 18 * (lane & 31)) * 9 + 3 * wp;
        gu_repair<0>(img); gu_repair<1>(img); gu_repair<2>(img); gu_repair<3>(img); gu_repair<4>(img);
        gu_repair<5>(img); gu_repair<6>(img); gu_repair<7>(img); gu_repair<8>(img);
        lds_claim(L_CTL + 12);
        waitvm<PTHR>(); ISSUE_Q(2, s.st2, s.seq2);
        ST1(39);
        while (ctl[3] < 6u && spin_more<1>(t0)) {}
        asm volatile("" ::: "memory");
        const __amdgpu_buffer_rsrc_t ro = __builtin_amdgcn_make_buffer_rsrc((void*)pp->wo, 0, QD * D * 2, 0x00020000);
        const __amdgpu_buffer_rsrc_t rd = __builtin_amdgcn_make_buffer_rsrc((void*)pp->wd, 0, F * D * 2, 0x00020000);
#pragma unroll
        for (int i = 0; i < 6; ++i) dma1k(vv, ro, cu * (16 * D * 2) + (6 * wp + i) * 1024, L_WO + (6 * wp + i) * 1024);
#pragma unroll
        for (int i = 0; i < 7; ++i) {
          const int pc = 7 * wp + i;
          if (pc < 20 || lane < 16) dma1k(vv, rd, mu * (18 * D * 2) + pc * 1024, L_WD + pc * 1024);
        }
        s.ops += 13;
      }
#undef PARK
    } else {
      s.seq0 = s.seq1 = s.seq2 = s.ops - 64;
      s.st0 = s.st1 = s.st2 = LIN_KEEP ? 5 : 1;
    }
    if (wave != 0) {
      const u64 t0 = __builtin_amdgcn_s_memrealtime();
      while (ctl[2] != (unsigned)(layer + 1) && spin_more<1>(t0)) {}
      asm volatile("" ::: "memory");
    }
    a.acc = __builtin_bit_cast(f32x4, zero4()); a.m = -INFINITY; a.l = asf(zero_u());
    STAMP(wave == 0 ? 3 + 12 * layer : (wave == 1 && layer == 0 ? 40 : 31));

    int e = wave == 0 ? 0 : 2;
#define WAIT_SLOT(SEQ) do { const int yg_ = s.ops - SEQ; if (yg_ >= 16) waitvm<16>(); else if (yg_ >= 8) waitvm<8>(); else waitvm<0>(); } while (0)
#define STEP(S, ST, SEQ) do { if (ST != 0) { \
      if ((ST & 4) == 0) { const QFrag q_ = q_frag<false>(qar); WAIT_SLOT(SEQ); tile_compute<S, false>(a, q_); } \
      bool lin_ = (ST & 4) != 0, lds_ = (LDSALL || S == 1) && s.nl != 0 && e >= LDSLAP; \
      if (lin_) WAIT_SLOT(SEQ); \
      while (lin_ || lds_) { \
        const QFrag q_ = q_frag<true>(qal); \
        if (!lin_) { tile_from_lds<S>(vv + s.ldsa); s.ldsa += 4 * 8192; --s.nl; lds_ = false; } \
        lin_ = false; \
        tile_compute<S, true>(a, q_); \
      } \
      if (s.next < nstream) { ISSUE_Q(S, ST, SEQ); PROG(); } \
      else if ((ST & 2) == 0 && layer == 0) { \
        tile_load<S, true, LIN_KEEP>(LIN_KEEP ? vk : vkr, vv, rk, rv, kv0 + (T_KEEP + 4 * S + wave) * 4096); s.ops += 8; SEQ = s.ops; ST = LIN_KEEP ? 7 : 3; } \
      else { ST = 0; } \
    } } while (0)
#define PROG() do {} while (0)
    for (;;) {
      STEP(0, s.st0, s.seq0);
      STEP(1, s.st1, s.seq1);
      STEP(2, s.st2, s.seq2);
      for (int r = 0; r < XPL; ++r, ++e) {
        if (e >= 11) continue;
        const QFrag q = q_frag<true>(qal);
        switch (e) {
          case 0: tile_compute<3, true>(a, q); break;
          case 1: tile_compute<4, true>(a, q); break;
          case 2: tile_compute<5, true>(a, q); break;
          case 3: tile_compute<6, true>(a, q); break;
          case 4: tile_compute<7, true>(a, q); break;
          case 5: tile_compute<8, true>(a, q); break;
          case 6: tile_compute<9, true>(a, q); break;
          case 7: tile_compute<10, true>(a, q); break;
          case 8: tile_compute<11, true>(a, q); break;
          case 9: tile_compute<12, true>(a, q); break;
          default: tile_compute<13, true>(a, q); break;
        }
      }
      if (e >= 11 && (s.st0 | s.st1 | s.st2) == 0) break;
    }
#undef STEP
#undef WAIT_SLOT
#undef PROG
#undef ISSUE_Q
    STAMP(wave == 0 ? 4 + 12 * layer : 27 + wave);

    {
      REFRESH();
      LAS float* mrg = (LAS float*)(lds + L_MRG) + 260 * wave;
      const float lsum = xrow_sum(a.l);
      const int h = (lane >> 3) & 1;
#pragma unroll
      for (int r = 0; r < 4; ++r) mrg[128 * h + 32 * (lane >> 4) + 8 * r + (lane & 7)] = a.acc[r];
      if ((lane & 55) == 0) { mrg[256 + 2 * h] = a.m; mrg[257 + 2 * h] = lsum; }
    }
    BAR();
    REFRESH();
    {
      LAS float* mrg = (LAS float*)(lds + L_MRG);
      const int tid = 64 * wave + lane, h = wave >> 1;
      float mw[4], M = -INFINITY;
#pragma unroll
      for (int w = 0; w < 4; ++w) { mw[w] = mrg[260 * w + 256 + 2 * h]; M = fmaxf(M, mw[w]); }
      float o = 0.f, L = 0.f;
#pragma unroll
      for (int w = 0; w < 4; ++w) { const float ew = __builtin_amdgcn_exp2f(mw[w] - M); o = fmaf(ew, mrg[260 * w + tid], o); L = fmaf(ew, mrg[260 * w + 257 + 2 * h], L); }
      __builtin_amdgcn_raw_buffer_store_b64(v2u{asu(o), tg + 1}, rw, rec + R_O + 8 * tid, 0, AUX_SC1);
      if ((tid & 127) == 0) __builtin_amdgcn_raw_buffer_store_b128(v4u{asu(M), tg + 1, asu(L), tg + 1}, rw, rec + R_ML + 16 * h, 0, AUX_SC1);
    }
    STAMP(wave == 0 ? 5 + 12 * layer : 31);
    if (wave == 0) {
      v4u g0 = zero4(), g1 = zero4(), ml = zero4();
      const int off = W_REC + (16 * grp + (lane >> 2)) * REC;
      const u64 t0 = __builtin_amdgcn_s_memrealtime();
      bool ok = false;
      do {
        if (!ok) {
          g0 = __builtin_amdgcn_raw_buffer_load_b128(rw, off + R_O + 128 * sp + 32 * (lane & 3), 0, AUX_SC1);
          g1 = __builtin_amdgcn_raw_buffer_load_b128(rw, off + R_O + 128 * sp + 32 * (lane & 3) + 16, 0, AUX_SC1);
          ml = __builtin_amdgcn_raw_buffer_load_b128(rw, off + R_ML + 16 * (sp >> 3), 0, AUX_SC1);
          ok = (g0[1] == tg + 1) & (g0[3] == tg + 1) & (g1[1] == tg + 1) & (g1[3] == tg + 1) & (ml[1] == tg + 1) & (ml[3] == tg + 1);
        }
      } while (!__all(ok) && spin_more<SL0>(t0));
      float M = asf(ml[0]);
      M = fmaxf(M, dpp<ROR4>(M)); M = fmaxf(M, dpp<ROR8>(M)); M = xrow_max(M);
      const float w = __builtin_amdgcn_exp2f(asf(ml[0]) - M);
      float v[5] = {w * asf(g0[0]), w * asf(g0[2]), w * asf(g1[0]), w * asf(g1[2]), w * asf(ml[2])};
#pragma unroll
      for (int k = 0; k < 5; ++k) { float t = v[k]; t += dpp<ROR4>(t); t += dpp<ROR8>(t); v[k] = xrow_sum(t); }
      const float il = __builtin_amdgcn_rcpf(v[4]);
      if (lane < 4) *(LAS v2u*)(lds + L_O + 8 * lane) = v2u{f2bf(v[0] * il) | (f2bf(v[1] * il) << 16), f2bf(v[2] * il) | (f2bf(v[3] * il) << 16)};
    }
    BAR();
    REFRESH();
    STAMP(wave == 0 ? 6 + 12 * layer : 31);
    {
      const u64 t0 = __builtin_amdgcn_s_memrealtime();
      if (wave != 0) rows_gemv_publish<2, 8>(lds, L_WO, L_O, wp, lane, rw, rec + R_Y, tg + 2);
      if (isred) {
        pause_until(t0, DLY_Y1);
        gather_y<SL_Y>(lds, rw, rho, wave, lane, tg + 2, t0);
        BAR();
        if (wave == 0) __builtin_amdgcn_raw_buffer_store_b64(v2u{finish_x(lds, lane), tg + 3}, rw, W_X + (lane >> 3) * XREP + 64 * rho + 8 * (lane & 7), 0, AUX_SC1);
        STAMP(wave == 0 ? 7 + 12 * layer : 31);
      }
      if (wave == 0) {
        REFRESH();
        v2u xp[3];
        pause_until(t0, DLY_X1);
        gather_x<SL_X>(rw, cu, lane, tg + 3, xp, t0);
        STAMP(8 + 12 * layer);
        layernorm_to_lds(xp, lds, lane);
      }
    }
    BAR();
    REFRESH();
    STAMP(wave == 0 ? 9 + 12 * layer : 31);
    if (wave != 0) {
      float acc[6];
#pragma unroll
      for (int k = 0; k < 6; ++k) acc[k] = asf(zero_u());
      LAS unsigned* ln2 = (LAS unsigned*)(lds + L_LN) + 9 * (lane & 31);
      unsigned in2[9];
#pragma unroll
      for (int i = 0; i < 9; ++i) in2[i] = ln2[i];
      gu_pair<0>(acc, in2[0]); gu_pair<1>(acc, in2[1]); gu_pair<2>(acc, in2[2]); gu_pair<3>(acc, in2[3]); gu_pair<4>(acc, in2[4]);
      gu_pair<5>(acc, in2[5]); gu_pair<6>(acc, in2[6]); gu_pair<7>(acc, in2[7]); gu_pair<8>(acc, in2[8]);
      asm volatile("s_nop 2" : "+v"(acc[0]), "+v"(acc[1]), "+v"(acc[2]), "+v"(acc[3]), "+v"(acc[4]), "+v"(acc[5]));
      unsigned hb[3];
#pragma unroll
      for (int k = 0; k < 3; ++k) {
        float t = swap16_add(acc[k], acc[k + 3]);
        t += dpp<XOR1>(t); t += dpp<XOR2>(t); t += dpp<ROR4>(t); t += dpp<ROR8>(t);
        auto sw = __builtin_amdgcn_permlane32_swap(asu(t), asu(t), false, false);
        const float g = rbf(asf(sw[0])), u = rbf(asf(sw[1]));
        hb[k] = f2bf((g > 0.f ? g : g * 0.f) * u);
      }
      if ((lane & 47) == 0) {
        LAS u16* h = (LAS u16*)(lds + L_H) + 6 * wp + 3 * (lane >> 4);
        h[0] = (u16)hb[0]; h[1] = (u16)hb[1]; h[2] = (u16)hb[2];
      }
    }
    BAR();
    REFRESH();
    STAMP(wave == 0 ? 10 + 12 * layer : 31);
    {
      const u64 t0 = __builtin_amdgcn_s_memrealtime();
      if (wave != 0) rows_gemv_publish<3, 9>(lds, L_WD, L_H, wp, lane, rw, rec + R_Y, tg + 4);
      if (isred) {
        pause_until(t0, DLY_Y2);
        gather_y<SL_Y>(lds, rw, rho, wave, lane, tg + 4, t0);
        BAR();
        if (wave == 0) {
          const unsigned xn = finish_x(lds, lane);
          if (layer == 0) {
            __builtin_amdgcn_raw_buffer_store_b64(v2u{xn, 5u}, rw, W_X + (lane >> 3) * XREP + 64 * rho + 8 * (lane & 7), 0, AUX_SC1);
          } else {
            const __amdgpu_buffer_rsrc_t ro = __builtin_amdgcn_make_buffer_rsrc((void*)kargs()->out, 0, D * 2, 0x00020000);
            if (lane < 8) __builtin_amdgcn_raw_buffer_store_b32(xn, ro, 2 * piece_col(4 * rho + (lane >> 1)) + 4 * (lane & 1), 0, 0);
            if (cu == 0 && lane == 0) { const u64 en = (((u64)ctl[5] << 32) | ctl[4]) + 2; __builtin_amdgcn_raw_buffer_store_b64(v2u{(unsigned)en, (unsigned)(en >> 32)}, rw, W_EPOCH, 0, AUX_SC1); }
          }
        }
        STAMP(wave == 0 ? 11 + 12 * layer : 31);
      }
      if (wave == 0 && layer == 0) {
        REFRESH();
        v2u xp[3];
        pause_until(t0, DLY_X2);
        gather_x<SL_X>(rw, cu, lane, 5, xp, t0);
        STAMP(12);
        layernorm_to_lds(xp, lds, lane);
      }
    }
  }
  asm volatile("" :: "a"(pin0), "a"(pin1));
}

#undef REFRESH
#undef grp
#undef sp
#undef wp
#undef mu
#undef kvb
#undef rec
#undef rho
#undef isred

}

extern "C" void kernel_launch(void* const* d_in, const int* in_sizes, int n_in, void* d_out, int out_size, void* d_ws, size_t ws_size,
                              hipStream_t stream) {
  Params p;
  p.x = (const u16*)d_in[0]; p.K = (const u16*)d_in[1]; p.V = (const u16*)d_in[2]; p.wq = (const u16*)d_in[3]; p.wo = (const u16*)d_in[4];
  p.wg = (const u16*)d_in[6]; p.wu = (const u16*)d_in[7]; p.wd = (const u16*)d_in[8];
  p.out = (u16*)d_out; p.ws = (unsigned char*)d_ws;
  (void)hipFuncSetAttribute((const void*)fused_decode, hipFuncAttributeMaxDynamicSharedMemorySize, LDS_BYTES);
  hipLaunchKernelGGL(fused_decode, dim3(NCU), dim3(256), LDS_BYTES, stream, p);
}
```

```cpp
#include <hip/hip_runtime.h>
#include <hip/hip_bf16.h>
#include <cstdint>

#define DIAG 0

namespace {

typedef unsigned v4u __attribute__((ext_vector_type(4)));
typedef unsigned v2u __attribute__((ext_vector_type(2)));
typedef float f32x4 __attribute__((ext_vector_type(4)));
typedef float f32x16 __attribute__((ext_vector_type(16)));
typedef float f32x32 __attribute__((ext_vector_type(32)));
typedef __bf16 bf16x2 __attribute__((ext_vector_type(2)));
typedef unsigned short u16;
typedef unsigned long long u64;
#define LAS __attribute__((address_space(3)))
#define GAS __attribute__((address_space(1)))

constexpr int D = 576, DH = 128, F = 4608, QD = 4096;
constexpr int NCU = 256;
constexpr float C2 = 0.088388347648318440f * 1.4426950408889634f;

constexpr int NLDS = 14;
constexpr int T_W0 = 36;
constexpr int T_LDS = 38;
constexpr int T_KEEP = T_LDS + NLDS;
constexpr int T_Q = T_KEEP + 12;
constexpr int NQ = 256 - T_Q;
#define NL2TILES 16
constexpr int NL2 = NL2TILES;
#define L2POS 3
constexpr int NPRE = L2POS;
#define ROWS3 4
constexpr int NROW3 = ROWS3, NROW4 = (NQ - 3 * NROW3) / 4;
static_assert(3 * NROW3 + 4 * NROW4 == NQ && NL2 % 4 == 0 && NQ % 4 == 0, "tile lists");

constexpr int L_TILES = 0;
constexpr int L_WO = NLDS * 8192;
constexpr int L_WD = L_WO + 16 * D * 2;
constexpr int L_MRG = L_WD + 18 * D * 2;
constexpr int L_LN = L_MRG + 4 * 260 * 4;
constexpr int L_Q = L_LN + D * 2;
constexpr int L_O = L_Q + 512;
constexpr int L_H = L_O + 64;
constexpr int L_QP = L_H + 64;
constexpr int L_CTL = L_QP + 256;
constexpr int L_END = L_CTL + 128;
constexpr int L_IMG = L_WO - 2 * 8192;
static_assert(L_IMG + 2 * D * 48 <= L_MRG, "gate/up image");
constexpr int LDS_BYTES = 163840;
static_assert(L_END <= LDS_BYTES, "LDS budget");

constexpr int W_EPOCH = 0;
constexpr int NRED = 36, NREP = 8, XREP = 2560;
constexpr int W_X = 4096;
constexpr int W_FLAG = 28672;
constexpr int W_Q = 32768;
constexpr int W_REC = 65536, REC = 7168;
constexpr int R_O = 0, R_Y = 2048, R_ML = R_Y + 4608;
constexpr int W_DIAG = W_REC + NCU * REC;
constexpr int W_END = W_DIAG + NCU * 64 * 8;
constexpr int AUX_SC1 = 16;

#define LDSLAP 8
#define LDSALL 0
#define XPL 1
#define PTHR 8
#define LIN_L2 true
#define L2PER 3
constexpr int L2MUL = 256 / L2PER + 1;
#define LIN_KEEP false
#define LIN_HEAD 0
#define LEVEL_STR 1
constexpr unsigned STRADDLE = 0x11224488u;
#define GUW 14
#define SL0 2
#define SL_Y 1
#define SL_X 1
#define SL_Q 1
#define DLY_Q 40
#define DLY_Y1 90
#define DLY_X1 220
#define DLY_Y2 90
#define DLY_X2 220

struct Params {
  const u16 *x, *K, *V, *wq, *wo, *wg, *wu, *wd;
  u16* out;
  unsigned char* ws;
};

__device__ __forceinline__ float rbf(float f) { return (float)(__bf16)f; }
__device__ __forceinline__ unsigned f2bf(float f) { return (unsigned)__builtin_bit_cast(u16, (__bf16)f); }
__device__ __forceinline__ float bf2f(unsigned b) { return __builtin_bit_cast(float, b << 16); }
__device__ __forceinline__ float asf(unsigned u) { return __builtin_bit_cast(float, u); }
__device__ __forceinline__ unsigned asu(float f) { return __builtin_bit_cast(unsigned, f); }
__device__ __forceinline__ float dot2(unsigned a, unsigned b, float c) {
  return __builtin_amdgcn_fdot2_f32_bf16(__builtin_bit_cast(bf16x2, a), __builtin_bit_cast(bf16x2, b), c, false);
}
template <int CTRL> __device__ __forceinline__ float dpp(float x) {
  return asf((unsigned)__builtin_amdgcn_mov_dpp((int)asu(x), CTRL, 0xf, 0xf, true));
}
constexpr int XOR1 = 0xB1, XOR2 = 0x4E, ROR4 = 0x124, ROR8 = 0x128;
__device__ __forceinline__ float swap16_add(float a, float b) {
  auto s = __builtin_amdgcn_permlane16_swap(asu(a), asu(b), false, false);
  return asf(s[0]) + asf(s[1]);
}
__device__ __forceinline__ float swap32_add(float a, float b) {
  auto s = __builtin_amdgcn_permlane32_swap(asu(a), asu(b), false, false);
  return asf(s[0]) + asf(s[1]);
}
__device__ __forceinline__ float xrow_max(float x) {
  auto s = __builtin_amdgcn_permlane16_swap(asu(x), asu(x), false, false);
  x = fmaxf(asf(s[0]), asf(s[1]));
  auto t = __builtin_amdgcn_permlane32_swap(asu(x), asu(x), false, false);
  return fmaxf(asf(t[0]), asf(t[1]));
}
__device__ __forceinline__ float xrow_sum(float x) { x = swap16_add(x, x); return swap32_add(x, x); }
__device__ __forceinline__ float wave_sum(float x) {
  x += dpp<XOR1>(x); x += dpp<XOR2>(x); x += dpp<ROR4>(x); x += dpp<ROR8>(x);
  return xrow_sum(x);
}

__device__ __forceinline__ int fresh_lane() { int l; asm volatile("v_mbcnt_lo_u32_b32 %0, -1, 0\n\tv_mbcnt_hi_u32_b32 %0, -1, %0" : "=v"(l)); return l; }
__device__ __forceinline__ unsigned zero_u() { unsigned z; asm volatile("v_mov_b32 %0, 0" : "=v"(z)); return z; }
__device__ __forceinline__ v4u zero4() { const unsigned z = zero_u(); return v4u{z, z, z, z}; }
__device__ __forceinline__ int fresh(int x) { asm volatile("" : "+s"(x)); return x; }
__device__ __forceinline__ const Params* kargs() {
  auto k = (const __attribute__((address_space(4))) Params*)__builtin_amdgcn_kernarg_segment_ptr();
  asm volatile("" : "+s"(k));
  return (const Params*)k;
}
#define BAR() asm volatile("s_waitcnt lgkmcnt(0)\n\ts_barrier" ::: "memory")
template <int N> __device__ __forceinline__ void waitvm() { asm volatile("s_waitcnt vmcnt(%c0)" :: "i"(N) : "memory"); }

template <int S> struct Slot { static constexpr bool acc = S >= 6; static constexpr int base = S >= 6 ? 32 * (S - 6) : 64 + 32 * S; };

#define LD1(P, R, VOFF, RS, OFF, POL) "buffer_load_dwordx4 " P "[%c0+" #R ":%c0+" #R "+3], " VOFF ", " RS ", %5 offen offset:" #OFF POL "\n\t"
#define LDV(P, POL) LD1(P, 16, "%2", "%4", 0, POL) LD1(P, 20, "%2", "%4", 1024, POL) LD1(P, 24, "%2", "%4", 2048, POL) LD1(P, 28, "%2", "%4", 3072, POL)
#define LD8R(P, POL) LD1(P, 0, "%1", "%3", 0, POL) LD1(P, 4, "%1", "%3", 64, POL) LD1(P, 8, "%1", "%3", 128, POL) LD1(P, 12, "%1", "%3", 192, POL) LDV(P, POL)
#define LD8L(P, POL) LD1(P, 0, "%1", "%3", 0, POL) LD1(P, 4, "%1", "%3", 1024, POL) LD1(P, 8, "%1", "%3", 2048, POL) LD1(P, 12, "%1", "%3", 3072, POL) LDV(P, POL)
#define LDOPS :: "i"(Slot<S>::base), "v"(vk), "v"(vv), "s"(rk), "s"(rv), "s"(soff) : "memory"
enum { P_KEEP = 0, P_NT = 1 };
template <int S, int POL, bool LIN>
__device__ __forceinline__ void tile_load(int vk, int vv, __amdgpu_buffer_rsrc_t rk, __amdgpu_buffer_rsrc_t rv, int soff) {
  if constexpr (Slot<S>::acc) {
    static_assert(POL == P_NT && LIN, "resident slots");
    asm volatile(LD8L("a", " nt") LDOPS);
  } else if constexpr (LIN) {
    static_assert(POL == P_NT, "linear tiles stream");
    asm volatile(LD8L("v", " nt") LDOPS);
  } else {
    if constexpr (POL == P_NT) asm volatile(LD8R("v", " nt") LDOPS); else asm volatile(LD8R("v", "") LDOPS);
  }
}
__device__ __forceinline__ int k_off_rows(int lane) { return (4 * (lane & 3) + ((lane >> 2) & 3)) * 256 + (lane >> 4) * 16; }
__device__ __forceinline__ int k_off_lin(int lane) { return ((lane >> 2) & 3) * 256 + (lane >> 4) * 64 + (lane & 3) * 16; }
__device__ __forceinline__ void dma1k(int voff, __amdgpu_buffer_rsrc_t rs, int soff, int m0v) {
  asm volatile("s_mov_b32 m0, %3\n\ts_nop 0\n\tbuffer_load_dwordx4 %0, %1, %2 offen nt lds" :: "v"(voff), "s"(rs), "s"(soff), "s"(m0v) : "memory");
}
__device__ __forceinline__ void tile_dma(int vk, int vv, __amdgpu_buffer_rsrc_t rk, __amdgpu_buffer_rsrc_t rv, int soff, int ldsb) {
#pragma unroll
  for (int i = 0; i < 4; ++i) dma1k(vk, rk, soff + 1024 * i, ldsb + 1024 * i);
#pragma unroll
  for (int i = 0; i < 4; ++i) dma1k(vv, rv, soff + 1024 * i, ldsb + 4096 + 1024 * i);
}
#define DR1(R, OFF) "ds_read_b128 v[%c0+" #R ":%c0+" #R "+3], %1 offset:" #OFF "\n\t"
template <int S> __device__ __forceinline__ void tile_from_lds(int addr) {
  static_assert(!Slot<S>::acc, "streaming slots only");
  asm volatile(DR1(0, 0) DR1(4, 1024) DR1(8, 2048) DR1(12, 3072) DR1(16, 4096) DR1(20, 5120) DR1(24, 6144) DR1(28, 7168) "s_waitcnt lgkmcnt(0)"
               :: "i"(Slot<S>::base), "v"(addr) : "memory");
}

struct Att { unsigned k0, k1, k2, k3; f32x4 acc; float m, l; };
struct QFrag { v4u q0, q1, q2, q3; };
template <bool LIN> __device__ __forceinline__ QFrag q_frag(int addr) {
  QFrag q;
  LAS v4u* p = (LAS v4u*)(uintptr_t)(unsigned)addr;
  q.q0 = p[0];
  if constexpr (!LIN) { q.q1 = p[4]; q.q2 = p[8]; q.q3 = p[12]; }
  return q;
}

#define QK4(P) "s_nop 1\n\tv_mfma_f32_16x16x32_bf16 %0, " P "[%c5:%c5+3], %1, 0\n\t" \
               "v_mfma_f32_16x16x32_bf16 %0, " P "[%c5+4:%c5+7], %2, %0\n\t" \
               "v_mfma_f32_16x16x32_bf16 %0, " P "[%c5+8:%c5+11], %3, %0\n\t" \
               "v_mfma_f32_16x16x32_bf16 %0, " P "[%c5+12:%c5+15], %4, %0\n\ts_nop 7"
#define QK4I(P) "s_nop 1\n\tv_mfma_f32_16x16x32_bf16 %0, " P "[%c5:%c5+3], %4, 0\n\t" \
                "v_mfma_f32_16x16x32_bf16 %1, " P "[%c5+4:%c5+7], %4, 0\n\t" \
                "v_mfma_f32_16x16x32_bf16 %2, " P "[%c5+8:%c5+11], %4, 0\n\t" \
                "v_mfma_f32_16x16x32_bf16 %3, " P "[%c5+12:%c5+15], %4, 0\n\ts_nop 7"
#define PV1(P) "s_nop 1\n\tv_mfma_f32_16x16x32_bf16 %0, " P "[%c2:%c2+3], %1, %0\n\ts_nop %c3"
template <int S, bool LIN> __device__ __forceinline__ void tile_compute(Att& a, const QFrag& q) {
  f32x4 s;
  if constexpr (LIN) {
    f32x4 d0, d1, d2, d3;
    if constexpr (Slot<S>::acc) asm volatile(QK4I("a") : "=&v"(d0), "=&v"(d1), "=&v"(d2), "=&v"(d3) : "v"(q.q0), "i"(Slot<S>::base));
    else asm volatile(QK4I("v") : "=&v"(d0), "=&v"(d1), "=&v"(d2), "=&v"(d3) : "v"(q.q0), "i"(Slot<S>::base));
    s[0] = (dpp<0x00>(d0[0]) + dpp<0x55>(d0[1])) + (dpp<0xAA>(d0[2]) + dpp<0xFF>(d0[3]));
    s[1] = (dpp<0x00>(d1[0]) + dpp<0x55>(d1[1])) + (dpp<0xAA>(d1[2]) + dpp<0xFF>(d1[3]));
    s[2] = (dpp<0x00>(d2[0]) + dpp<0x55>(d2[1])) + (dpp<0xAA>(d2[2]) + dpp<0xFF>(d2[3]));
    s[3] = (dpp<0x00>(d3[0]) + dpp<0x55>(d3[1])) + (dpp<0xAA>(d3[2]) + dpp<0xFF>(d3[3]));
  } else {
    static_assert(LIN || !Slot<S>::acc, "resident slots are linear");
    asm volatile(QK4("v") : "=&v"(s) : "v"(q.q0), "v"(q.q1), "v"(q.q2), "v"(q.q3), "i"(Slot<S>::base));
  }
  const float mx = xrow_max(fmaxf(fmaxf(s[0], s[1]), fmaxf(s[2], s[3])));
  const float mn = fmaxf(a.m, mx * C2);
  const float alpha = __builtin_amdgcn_exp2f(a.m - mn);
  a.m = mn;
  a.acc *= alpha;
  float ps = 0.f;
#pragma unroll
  for (int t = 0; t < 4; ++t) {
    const float p = __builtin_amdgcn_exp2f(fmaf(s[t], C2, -mn));
    ps += p;
    bf16x2 pb = {(__bf16)p, (__bf16)p};
    const unsigned pp = __builtin_bit_cast(unsigned, pb);
    const v4u b = {pp & a.k0, pp & a.k1, pp & a.k2, pp & a.k3};
    if constexpr (Slot<S>::acc) asm volatile(PV1("a") : "+v"(a.acc) : "v"(b), "i"(Slot<S>::base + 16 + 4 * t), "i"(t == 3 ? 7 : 0));
    else asm volatile(PV1("v") : "+v"(a.acc) : "v"(b), "i"(Slot<S>::base + 16 + 4 * t), "i"(t == 3 ? 7 : 0));
  }
  a.l = fmaf(a.l, alpha, ps);
}

__device__ __forceinline__ int lds_claim(int addr) {
  int t; unsigned tmp; const unsigned one = 1;
  asm volatile("s_mov_b64 exec, 1\n\tds_add_rtn_u32 %1, %2, %3\n\ts_waitcnt lgkmcnt(0)\n\tv_readfirstlane_b32 %0, %1\n\ts_mov_b64 exec, -1"
               : "=s"(t), "=&v"(tmp) : "v"(addr), "v"(one) : "memory");
  return t;
}
constexpr unsigned SEL_LO = 0x05040100u, SEL_HI = 0x07060302u;
__device__ __forceinline__ unsigned perm2(unsigned a, unsigned b, unsigned sel) { return __builtin_amdgcn_perm(b, a, sel); }

constexpr unsigned SPIN_CAP = 1u << 21;
template <int SLEEP> __device__ __forceinline__ bool spin_more(unsigned& n) {
  __builtin_amdgcn_s_sleep(SLEEP);
  n = (unsigned)fresh((int)n + 1);
  return n < SPIN_CAP;
}
__device__ __forceinline__ void pause_until(u64 t0, unsigned ticks) {
  while ((unsigned)(__builtin_amdgcn_s_memrealtime() - t0) < ticks) __builtin_amdgcn_s_sleep(1);
}

__device__ __forceinline__ int piece_col(int P) { return P < 96 ? 12 * (P >> 1) + 4 * (P & 1) : 12 * (P - 96) + 8; }

__device__ __forceinline__ void layernorm_to_lds(const v2u (&xp)[3], LAS unsigned char* lds, int lane) {
  const bool live = lane < 48;
  float x[12];
#pragma unroll
  for (int k = 0; k < 3; ++k) {
    x[4 * k] = asf(xp[k][0] << 16); x[4 * k + 1] = asf(xp[k][0] & 0xffff0000u); x[4 * k + 2] = asf(xp[k][1] << 16); x[4 * k + 3] = asf(xp[k][1] & 0xffff0000u);
  }
  float s = 0.f;
#pragma unroll
  for (int i = 0; i < 12; ++i) s += x[i];
  const float mean = wave_sum(live ? s : 0.f) * (1.f / D);
  float v = 0.f;
#pragma unroll
  for (int i = 0; i < 12; ++i) { const float d = x[i] - mean; v = fmaf(d, d, v); }
  const float var = rbf(wave_sum(live ? v : 0.f) * (1.f / D));
  const float mb = rbf(mean), inv = __builtin_amdgcn_rcpf(rbf(__builtin_amdgcn_sqrtf(rbf(var + 1e-5f))));
  if (live) {
#pragma unroll
    for (int k = 0; k < 3; ++k) {
      bf16x2 lo = {(__bf16)(rbf(x[4 * k] - mb) * inv), (__bf16)(rbf(x[4 * k + 1] - mb) * inv)};
      bf16x2 hi = {(__bf16)(rbf(x[4 * k + 2] - mb) * inv), (__bf16)(rbf(x[4 * k + 3] - mb) * inv)};
      *(LAS v2u*)(lds + L_LN + 2 * piece_col(lane + 48 * k)) = v2u{__builtin_bit_cast(unsigned, lo), __builtin_bit_cast(unsigned, hi)};
    }
  }
}

template <int NP, int NROWPAIRS>
__device__ __forceinline__ void rows_gemv_publish(LAS unsigned char* lds, int wbase, int inbase, int wp, int lane,
                                                  __amdgpu_buffer_rsrc_t rw, int ybase, unsigned tag) {
  const int rg = lane >> 4, c = lane & 15;
  float acc[12];
#pragma unroll
  for (int k = 0; k < 12; ++k) acc[k] = asf(zero_u());
#pragma unroll
  for (int i = 0; i < NP; ++i) {
    const int p = rg + 4 * i, pw = p < NROWPAIRS ? p : NROWPAIRS - 1;
    const unsigned in2 = *(LAS unsigned*)(lds + inbase + 4 * p);
    LAS v2u* r0 = (LAS v2u*)(lds + wbase + (2 * pw) * (D * 2) + 384 * wp + 24 * c);
    LAS v2u* r1 = (LAS v2u*)(lds + wbase + (2 * pw + 1) * (D * 2) + 384 * wp + 24 * c);
#pragma unroll
    for (int j = 0; j < 3; ++j) {
      const v2u a = r0[j], b = r1[j];
      acc[4 * j + 0] = dot2(perm2(a[0], b[0], SEL_LO), in2, acc[4 * j + 0]);
      acc[4 * j + 1] = dot2(perm2(a[0], b[0], SEL_HI), in2, acc[4 * j + 1]);
      acc[4 * j + 2] = dot2(perm2(a[1], b[1], SEL_LO), in2, acc[4 * j + 2]);
      acc[4 * j + 3] = dot2(perm2(a[1], b[1], SEL_HI), in2, acc[4 * j + 3]);
    }
  }
  const float y0 = swap32_add(swap16_add(acc[0], acc[2]), swap16_add(acc[4], acc[6]));
  const float y1 = swap32_add(swap16_add(acc[1], acc[3]), swap16_add(acc[5], acc[7]));
  const float y2 = swap32_add(swap16_add(acc[8], acc[9]), swap16_add(acc[10], acc[11]));
  const int idx = 64 * wp + 4 * c + rg;
  __builtin_amdgcn_raw_buffer_store_b128(v4u{asu(y0), tag, asu(y1), tag}, rw, ybase + 16 * idx, 0, AUX_SC1);
  __builtin_amdgcn_raw_buffer_store_b64(v2u{asu(y2), tag}, rw, ybase + 3072 + 8 * idx, 0, AUX_SC1);
}

template <int SLEEP>
__device__ __forceinline__ void gather_y(LAS unsigned char* lds, __amdgpu_buffer_rsrc_t rw, int rho, int wave, int lane, unsigned tag) {
  unsigned spins = 0;
  const int off = W_REC + (64 * wave + (lane >> 3)) * REC + R_Y + 128 * rho + 16 * (lane & 7);
  float s0, s1;
  bool ok;
  do {
    v4u a[8];
#pragma unroll
    for (int k = 0; k < 8; ++k) a[k] = __builtin_amdgcn_raw_buffer_load_b128(rw, off, k * 8 * REC, AUX_SC1);
    ok = true; s0 = 0.f; s1 = 0.f;
#pragma unroll
    for (int k = 0; k < 8; ++k) { ok &= (a[k][1] == tag) & (a[k][3] == tag); s0 += asf(a[k][0]); s1 += asf(a[k][2]); }
  } while (!__all(ok) && spin_more<SLEEP>(spins));
  s0 += dpp<ROR8>(s0); s1 += dpp<ROR8>(s1);
  s0 = xrow_sum(s0); s1 = xrow_sum(s1);
  if (lane < 8) *(LAS v2u*)(lds + L_QP + 64 * wave + 8 * lane) = v2u{asu(s0), asu(s1)};
}
__device__ __forceinline__ unsigned finish_x(LAS unsigned char* lds, int lane) {
  const int j = lane & 7;
  float s0 = 0.f, s1 = 0.f;
#pragma unroll
  for (int w = 0; w < 4; ++w) { const v2u t = *(LAS v2u*)(lds + L_QP + 64 * w + 8 * j); s0 += asf(t[0]); s1 += asf(t[1]); }
  LAS unsigned* res = (LAS unsigned*)(lds + L_CTL + 64) + j;
  const unsigned r = *res;
  bf16x2 xb = {(__bf16)(asf(r << 16) + rbf(s0)), (__bf16)(asf(r & 0xffff0000u) + rbf(s1))};
  const unsigned xn = __builtin_bit_cast(unsigned, xb);
  asm volatile("s_waitcnt lgkmcnt(0)" ::: "memory");
  *res = xn;
  return xn;
}
template <int SLEEP>
__device__ __forceinline__ void gather_x(__amdgpu_buffer_rsrc_t rw, int cu, int lane, unsigned tag, v2u (&xp)[3]) {
  unsigned spins = 0;
  v4u g[3] = {zero4(), zero4(), zero4()};
  const int off = W_X + (cu & (NREP - 1)) * XREP + 16 * (lane < 48 ? lane : 47);
  bool ok = false;
  do {
    if (!ok) {
#pragma unroll
      for (int k = 0; k < 3; ++k) g[k] = __builtin_amdgcn_raw_buffer_load_b128(rw, off + 768 * k, 0, AUX_SC1);
      ok = (g[0][1] == tag) & (g[0][3] == tag) & (g[1][1] == tag) & (g[1][3] == tag) & (g[2][1] == tag) & (g[2][3] == tag);
    }
  } while (!__all(ok) && spin_more<SLEEP>(spins));
#pragma unroll
  for (int k = 0; k < 3; ++k) xp[k] = v2u{g[k][0], g[k][2]};
}

#define STAMP(i) do {} while (0)
#define ST1(i) do {} while (0)

template <int K> __device__ __forceinline__ void wq_load_one(int voff, __amdgpu_buffer_rsrc_t rs) {
  asm volatile("buffer_load_dwordx4 v[%c0:%c0+3], %1, %2, %3 offen" :: "i"(136 + 4 * K), "v"(voff), "s"(rs), "s"(K * QD * 2) : "memory");
}
__device__ __forceinline__ void dma1k_shared(int voff, __amdgpu_buffer_rsrc_t rs, int soff, int m0v) {
  asm volatile("s_mov_b32 m0, %3\n\ts_nop 0\n\tbuffer_load_dwordx4 %0, %1, %2 offen lds" :: "v"(voff), "s"(rs), "s"(soff), "s"(m0v) : "memory");
}
constexpr int wq_reg(int j) { return j < 10 ? 214 + j : 48 + (j - 10); }
template <int DST0, int DST1, int RA, int RB> __device__ __forceinline__ void pair_regs() {
  asm volatile("v_perm_b32 v%c0, v%c3, v%c2, %4\n\tv_perm_b32 v%c1, v%c3, v%c2, %5" :: "i"(DST0), "i"(DST1), "i"(RA), "i"(RB), "s"(SEL_LO), "s"(SEL_HI));
}
template <int DST> __device__ __forceinline__ void pair_vals(unsigned a, unsigned b) {
  asm volatile("v_perm_b32 v%c0, %2, %1, %3\n\tv_perm_b32 v[%c0+1], %2, %1, %4" :: "i"(DST), "v"(a), "v"(b), "s"(SEL_LO), "s"(SEL_HI));
}
template <int I> __device__ __forceinline__ void gu_repair(LAS unsigned* img) {
  const unsigned a0 = img[24 * I], a1 = img[24 * I + 1], a2 = img[24 * I + 2], b0 = img[24 * I + 12], b1 = img[24 * I + 13], b2 = img[24 * I + 14];
  pair_vals<160 + 6 * I>(a0, b0); pair_vals<162 + 6 * I>(a1, b1); pair_vals<164 + 6 * I>(a2, b2);
}
template <int I> __device__ __forceinline__ void wq_repair() {
  pair_regs<wq_reg(8 * I + 0), wq_reg(8 * I + 1), 136 + 8 * I + 0, 140 + 8 * I + 0>();
  pair_regs<wq_reg(8 * I + 2), wq_reg(8 * I + 3), 136 + 8 * I + 1, 140 + 8 * I + 1>();
  pair_regs<wq_reg(8 * I + 4), wq_reg(8 * I + 5), 136 + 8 * I + 2, 140 + 8 * I + 2>();
  pair_regs<wq_reg(8 * I + 6), wq_reg(8 * I + 7), 136 + 8 * I + 3, 140 + 8 * I + 3>();
}
template <int R0, int R1> __device__ __forceinline__ void dot_regs(float& a0, float& a1, unsigned in2) {
  asm volatile("v_dot2c_f32_bf16 %0, v%c2, %4\n\tv_dot2c_f32_bf16 %1, v%c3, %4" : "+v"(a0), "+v"(a1) : "i"(R0), "i"(R1), "v"(in2));
}
template <int I> __device__ __forceinline__ void gu_pair(float (&acc)[6], unsigned in2) {
  dot_regs<160 + 6 * I + 0, 160 + 6 * I + 1>(acc[0], acc[1], in2);
  dot_regs<160 + 6 * I + 2, 160 + 6 * I + 3>(acc[2], acc[3], in2);
  dot_regs<160 + 6 * I + 4, 160 + 6 * I + 5>(acc[4], acc[5], in2);
}
template <int I> __device__ __forceinline__ void wq_pair(float (&acc)[8], unsigned in2) {
  dot_regs<wq_reg(8 * I + 0), wq_reg(8 * I + 1)>(acc[0], acc[1], in2);
  dot_regs<wq_reg(8 * I + 2), wq_reg(8 * I + 3)>(acc[2], acc[3], in2);
  dot_regs<wq_reg(8 * I + 4), wq_reg(8 * I + 5)>(acc[4], acc[5], in2);
  dot_regs<wq_reg(8 * I + 6), wq_reg(8 * I + 7)>(acc[6], acc[7], in2);
}

__device__ __forceinline__ void wowd_dma(int cu, int mu, int wp, int vv) {
  const Params* pp = kargs();
  const __amdgpu_buffer_rsrc_t ro = __builtin_amdgcn_make_buffer_rsrc((void*)pp->wo, 0, QD * D * 2, 0x00020000);
  const __amdgpu_buffer_rsrc_t rd = __builtin_amdgcn_make_buffer_rsrc((void*)pp->wd, 0, F * D * 2, 0x00020000);
#pragma unroll
  for (int i = 0; i < 6; ++i) dma1k(vv, ro, cu * (16 * D * 2) + (6 * wp + i) * 1024, L_WO + (6 * wp + i) * 1024);
#pragma unroll
  for (int i = 0; i < 7; ++i) {
    const int pc = 7 * wp + i;
    if (pc < 20 || vv < 256) dma1k(vv, rd, mu * (18 * D * 2) + pc * 1024, L_WD + pc * 1024);
  }
}

struct Stream { int st0, st1, st2, seq0, seq1, seq2, ops, next, nl, ldsa; };

__global__ void __attribute__((amdgpu_num_vgpr(48))) __launch_bounds__(256, 1) fused_decode(Params p) {
  extern __shared__ __attribute__((aligned(16))) unsigned char lds_raw[];
  LAS unsigned char* lds = (LAS unsigned char*)lds_raw;
  asm volatile("" ::: "v255", "a255");
  f32x32 pin0; f32x16 pin1;
  asm volatile("" : "=a"(pin0), "=a"(pin1));
  int wave = __builtin_amdgcn_readfirstlane(threadIdx.x >> 6), cu = blockIdx.x, lane = fresh_lane();
#define REFRESH() do { lane = fresh_lane(); cu = fresh(cu); wave = fresh(wave); } while (0)
#define grp (cu >> 4)
#define sp (cu & 15)
#define wp (wave - 1)
#define mu ((cu & 7) * 32 + (cu >> 3))
#define kvb (cu << 20)
#define rec (W_REC + cu * REC)
#define rho ((cu * 9363) >> 16)
#define isred (cu == 7 * rho && cu < 7 * NRED)
  const __amdgpu_buffer_rsrc_t rk = __builtin_amdgcn_make_buffer_rsrc((void*)p.K, 0, 1 << 28, 0x00020000);
  const __amdgpu_buffer_rsrc_t rv = __builtin_amdgcn_make_buffer_rsrc((void*)p.V, 0, 1 << 28, 0x00020000);
  const __amdgpu_buffer_rsrc_t rw = __builtin_amdgcn_make_buffer_rsrc((void*)p.ws, 0, W_END, 0x00020000);
  volatile LAS unsigned* ctl = (volatile LAS unsigned*)(lds + L_CTL);
  STAMP(wave == 0 ? 0 : 31);

  if (wave == 0) {
    const v2u e = __builtin_amdgcn_raw_buffer_load_b64(rw, W_EPOCH, 0, AUX_SC1);
    v2u xp[3];
#pragma unroll
    for (int k = 0; k < 3; ++k) xp[k] = *(const GAS v2u*)(p.x + piece_col((lane < 48 ? lane : 47) + 48 * k));
    const unsigned xr = *(const GAS unsigned*)(p.x + piece_col(4 * (isred ? rho : 0) + ((lane & 7) >> 1)) + 2 * (lane & 1));
#pragma unroll
    for (int k = 0; k < 7; ++k) __builtin_amdgcn_raw_buffer_store_b128(zero4(), rw, rec + 1024 * k + 16 * lane, 0, AUX_SC1);
    if (isred) __builtin_amdgcn_raw_buffer_store_b64(v2u{0, 0}, rw, W_X + (lane >> 3) * XREP + 64 * rho + 8 * (lane & 7), 0, AUX_SC1);
    if (lane < 4) ctl[lane] = 0u;
    if (lane < 16) *(LAS unsigned*)(lds + L_H + 4 * lane) = 0u;
    if (lane < 8) ctl[16 + lane] = xr;
    if (lane == 0) { ctl[4] = e[0]; ctl[5] = e[1]; }
    layernorm_to_lds(xp, lds, lane);
    waitvm<0>();
    const u64 tag = ((((u64)e[1] << 32) | e[0]) + 1) << 16;
    if (lane == 0) __builtin_amdgcn_raw_buffer_store_b64(v2u{(unsigned)tag, (unsigned)(tag >> 32)}, rw, W_FLAG + 8 * cu, 0, AUX_SC1);
  } else {
    ST1(32);
    const int vv = lane * 16, vk = k_off_lin(lane);
    const __amdgpu_buffer_rsrc_t rq = __builtin_amdgcn_make_buffer_rsrc((void*)p.wq, 0, D * QD * 2, 0x00020000);
    const int vq = (192 * wp + 6 * (lane >> 1)) * (QD * 2) + 32 * mu + 16 * (lane & 1);
    tile_dma(vk, vv, rk, rv, kvb + (T_LDS + wave) * 4096, L_TILES + wave * 8192);
    wq_load_one<0>(vq, rq);
    tile_dma(vk, vv, rk, rv, kvb + (T_LDS + wave + 4) * 4096, L_TILES + (wave + 4) * 8192);
    wq_load_one<1>(vq, rq);
    ST1(33);
    waitvm<PTHR + 2>(); tile_dma(vk, vv, rk, rv, kvb + (T_LDS + wave + 8) * 4096, L_TILES + (wave + 8) * 8192);
    wq_load_one<2>(vq, rq);
    waitvm<PTHR + 2>(); tile_load<5, P_NT, true>(vk, vv, rk, rv, kvb + wave * 4096);
    wq_load_one<3>(vq, rq);
    waitvm<PTHR + 2>(); tile_load<6, P_NT, true>(vk, vv, rk, rv, kvb + (4 + wave) * 4096);
    wq_load_one<4>(vq, rq);
    waitvm<PTHR + 2>(); tile_load<7, P_NT, true>(vk, vv, rk, rv, kvb + (8 + wave) * 4096);
    wq_load_one<5>(vq, rq);
    waitvm<PTHR + 2>(); tile_load<8, P_NT, true>(vk, vv, rk, rv, kvb + (12 + wave) * 4096);
    ST1(34);
  }

  Stream s;
  s.ops = 0;

  for (int layer = 0; layer < 2; ++layer) {
    const unsigned tg = 4 * layer;
    BAR();
    REFRESH();
    if (wave != 0) {
      if (layer == 0) { waitvm<8>(); wq_repair<0>(); wq_repair<1>(); wq_repair<2>(); }
      float acc[8];
#pragma unroll
      for (int k = 0; k < 8; ++k) acc[k] = asf(zero_u());
      LAS unsigned* ln2 = (LAS unsigned*)(lds + L_LN) + 96 * wp + 3 * (lane >> 1);
      const unsigned i0 = ln2[0], i1 = ln2[1], i2 = ln2[2];
      wq_pair<0>(acc, i0); wq_pair<1>(acc, i1); wq_pair<2>(acc, i2);
      asm volatile("s_nop 2" : "+v"(acc[0]), "+v"(acc[1]), "+v"(acc[2]), "+v"(acc[3]), "+v"(acc[4]), "+v"(acc[5]), "+v"(acc[6]), "+v"(acc[7]));
      float u[2];
#pragma unroll
      for (int k = 0; k < 2; ++k) {
        float t = swap16_add(swap32_add(acc[k], acc[k + 4]), swap32_add(acc[k + 2], acc[k + 6]));
        t += dpp<ROR8>(t); t += dpp<ROR4>(t); t += dpp<XOR2>(t);
        u[k] = t;
      }
      if ((lane & 14) == 0) {
        LAS float* qp = (LAS float*)(lds + L_QP) + 16 * wp + 8 * (lane & 1) + 4 * (lane >> 5) + 2 * ((lane >> 4) & 1);
        qp[0] = u[0]; qp[1] = u[1];
      }
    }
    BAR();
    REFRESH();
    STAMP(wave == 0 ? 1 + 12 * layer : 31);
    if (wave == 0) {
      const u64 epoch = ((u64)(unsigned)__builtin_amdgcn_readfirstlane(ctl[5]) << 32) | (unsigned)__builtin_amdgcn_readfirstlane(ctl[4]);
      const u64 tag = (epoch + 1 + layer) << 16;
      if (lane < 16) {
        LAS float* qp = (LAS float*)(lds + L_QP) + lane;
        const u64 g = tag | f2bf((qp[0] + qp[16]) + qp[32]);
        __builtin_amdgcn_raw_buffer_store_b64(v2u{(unsigned)g, (unsigned)(g >> 32)}, rw, W_Q + mu * 128 + 8 * lane, 0, AUX_SC1);
      }
      const unsigned tlo = (unsigned)tag >> 16, thi = (unsigned)(tag >> 32);
      {
        v4u g0 = zero4(), g1 = zero4();
        const u64 t0 = __builtin_amdgcn_s_memrealtime();
        bool ok = false;
        unsigned spins = 0;
        pause_until(t0, DLY_Q);
        do {
          if (!ok) {
            g0 = __builtin_amdgcn_raw_buffer_load_b128(rw, W_Q + 2048 * grp + 32 * lane, 0, AUX_SC1);
            g1 = __builtin_amdgcn_raw_buffer_load_b128(rw, W_Q + 2048 * grp + 32 * lane + 16, 0, AUX_SC1);
            ok = ((g0[0] >> 16) == tlo) & (g0[1] == thi) & ((g0[2] >> 16) == tlo) & (g0[3] == thi) &
                 ((g1[0] >> 16) == tlo) & (g1[1] == thi) & ((g1[2] >> 16) == tlo) & (g1[3] == thi);
          }
        } while (!__all(ok) && spin_more<SL_Q>(spins));
        *(LAS v2u*)(lds + L_Q + 8 * lane) = v2u{perm2(g0[0], g0[2], SEL_LO), perm2(g1[0], g1[2], SEL_LO)};
        asm volatile("s_waitcnt lgkmcnt(0)" ::: "memory");
        ctl[2] = layer + 1;
        STAMP(layer == 0 ? 24 : 31);
      }
      if (layer == 0) {
        unsigned spins = 0;
        bool ok;
        do {
          const v4u f0 = __builtin_amdgcn_raw_buffer_load_b128(rw, W_FLAG + 16 * lane, 0, AUX_SC1);
          const v4u f1 = __builtin_amdgcn_raw_buffer_load_b128(rw, W_FLAG + 1024 + 16 * lane, 0, AUX_SC1);
          ok = (f0[0] == tlo << 16) & (f0[1] == thi) & (f0[2] == tlo << 16) & (f0[3] == thi) &
               (f1[0] == tlo << 16) & (f1[1] == thi) & (f1[2] == tlo << 16) & (f1[3] == thi);
        } while (!__all(ok) && spin_more<32>(spins));
      }
    }
    STAMP(wave == 0 ? 2 + 12 * layer : 31);
    REFRESH();
    const int kv0 = kvb;
    const int vk = k_off_lin(lane), vkr = k_off_rows(lane), vv = lane * 16;
    const int qal = L_Q + (lane & 8) * 32 + (lane >> 4) * 64 + (lane & 3) * 16, qar = L_Q + (lane & 8) * 32 + (lane >> 4) * 16;
    int x0, x1, x2, x3, nx = 0;
    {
      const int r0 = (T_Q + (layer == 0 ? 3 * NROW3 : 0) + wave) * 4096, r1 = r0 + 4 * 4096;
      const bool str = LEVEL_STR && layer == 0 && wave != 0;
      const bool sgive = (STRADDLE >> (cu >> 3)) & 1, stake = (STRADDLE >> ((cu >> 3) ^ 1)) & 1;
      x0 = x1 = x2 = x3 = 0;
      auto push = [&](int so) { x3 = x2; x2 = x1; x1 = x0; x0 = so; ++nx; };
      push(kv0 + r0);
      if (!(str && sgive)) push(kv0 + r1);
      if (str && stake) push(((cu ^ 8) << 20) + r1);
    }
    const int nstream = nx - 2 + (layer == 0 ? (wave == 0 ? NROW4 : NROW4 + NROW3) : NQ / 4);
    auto next_off = [&](int n) -> int {
      if (n < nx) return n == 0 ? x0 : n == 1 ? x1 : n == 2 ? x2 : x3;
      const int i = n - nx;
      if (layer == 0) return kv0 + 4096 * (T_Q + (wave == 0 ? 3 * NROW3 + 8 + 4 * i : (i < NROW3 ? 3 * i + wave - 1 : 4 * i - NROW3 + 8 + wave)));
      const int k = i - NPRE, t = (k * L2MUL) >> 8, r = k - L2PER * t;
      const int row = k < 0 ? i + 2 : (k >= L2PER * (NL2 / 4) ? i + 2 - NL2 / 4 : (r == L2PER - 1 ? NQ / 4 - NL2 / 4 + t : i + 2 - t));
      return kv0 + 4096 * (T_Q + wave + 4 * row);
    };
#define ISSUE_Q(S, ST, SEQ) do { const int so_ = next_off(s.next), id_ = (so_ >> 12) & 255; \
      const bool lin_ = layer == 0 ? s.next < LIN_HEAD : (LIN_L2 && (unsigned)(s.next - nx - NPRE) < (unsigned)(L2PER * (NL2 / 4))); \
      if (layer == 0 && id_ >= 256 - NL2) tile_load<S, P_KEEP, false>(vkr, vv, rk, rv, so_); \
      else if (lin_) tile_load<S, P_NT, true>(vk, vv, rk, rv, so_); \
      else tile_load<S, P_NT, false>(vkr, vv, rk, rv, so_); \
      ST = lin_ ? 5 : 1; \
      ++s.next; s.ops += 8; SEQ = s.ops; } while (0)
    Att a;
    a.k0 = ((lane & 7) >> 1) == 0 ? ((lane & 1) ? 0xffff0000u : 0xffffu) : 0u;
    a.k1 = ((lane & 7) >> 1) == 1 ? ((lane & 1) ? 0xffff0000u : 0xffffu) : 0u;
    a.k2 = ((lane & 7) >> 1) == 2 ? ((lane & 1) ? 0xffff0000u : 0xffffu) : 0u;
    a.k3 = ((lane & 7) >> 1) == 3 ? ((lane & 1) ? 0xffff0000u : 0xffffu) : 0u;
    s.nl = wave < 2 ? 4 : 3;
    s.ldsa = L_TILES + wave * 8192;
    s.next = 0;
#define IMG_DONE() do { unsigned n_ = 0; while (ctl[3] < 6u && spin_more<1>(n_)) {} } while (0)
    if (layer == 0) {
#define PARK(S) do { waitvm<PTHR>(); tile_load<S, P_NT, true>(vk, vv, rk, rv, kv0 + (4 * (S - 5) + wave) * 4096); } while (0)
      if (wave == 0) {
        waitvm<PTHR>(); tile_load<3, P_NT, true>(vk, vv, rk, rv, kv0 + T_W0 * 4096);
        waitvm<PTHR>(); tile_load<4, P_NT, true>(vk, vv, rk, rv, kv0 + (T_W0 + 1) * 4096);
      }
      if (wave == 0) {
        for (int k = 0; k < 3; ++k) { waitvm<PTHR>(); tile_dma(vk, vv, rk, rv, kv0 + (T_LDS + 4 * k) * 4096, L_TILES + 4 * k * 8192); }
        PARK(5); PARK(6); PARK(7); PARK(8); PARK(9); PARK(10); PARK(11); PARK(12); PARK(13);
        IMG_DONE();
        waitvm<PTHR>(); tile_dma(vk, vv, rk, rv, kv0 + (T_LDS + 12) * 4096, L_TILES + 12 * 8192);
        waitvm<PTHR>(); ISSUE_Q(0, s.st0, s.seq0);
        waitvm<PTHR>(); ISSUE_Q(1, s.st1, s.seq1);
        waitvm<PTHR>(); ISSUE_Q(2, s.st2, s.seq2);
      } else {
        const Params* pp = kargs();
        {
          const __amdgpu_buffer_rsrc_t rg = __builtin_amdgcn_make_buffer_rsrc((void*)pp->wg, 0, D * F * 2, 0x00020000);
          const __amdgpu_buffer_rsrc_t ru = __builtin_amdgcn_make_buffer_rsrc((void*)pp->wu, 0, D * F * 2, 0x00020000);
          int vo[3];
#pragma unroll
          for (int k = 0; k < 3; ++k) { const int j = 64 * k + lane, row = (j * 171) >> 9; vo[k] = row * (F * 2) + 16 * (j - 3 * row); }
#define GUGRP(RS, MAT, G) do { _Pragma("unroll") for (int k = 0; k < 3; ++k) \
            dma1k_shared(vo[k], RS, (192 * wp + 64 * G) * (F * 2) + ((36 * mu) & ~15), L_IMG + (MAT * D + 192 * wp + 64 * G) * 48 + 1024 * k); } while (0)
#define PARKD(S) do { waitvm<GUW>(); tile_load<S, P_NT, true>(vk, vv, rk, rv, kv0 + (4 * (S - 5) + wave) * 4096); } while (0)
          ST1(35);
          GUGRP(rg, 0, 0); PARKD(9); GUGRP(rg, 0, 1); PARKD(10); GUGRP(rg, 0, 2); PARKD(11);
          ST1(36);
          GUGRP(ru, 1, 0); PARKD(12); GUGRP(ru, 1, 1); PARKD(13); GUGRP(ru, 1, 2);
#undef PARKD
#undef GUGRP
        }
        ST1(37);
        waitvm<GUW>(); ISSUE_Q(0, s.st0, s.seq0);
        waitvm<GUW - 3>(); ISSUE_Q(1, s.st1, s.seq1);
        waitvm<16>();
        lds_claim(L_CTL + 12);
        unsigned spins = 0;
        while (ctl[3] < 3u && spin_more<1>(spins)) {}
        asm volatile("" ::: "memory");
        ST1(38);
        LAS unsigned* img = (LAS unsigned*)(lds + L_IMG) + ((lane >> 5) * D + 18 * (lane & 31)) * 12 + ((9 * mu) & 3) + 3 * wp;
        gu_repair<0>(img); gu_repair<1>(img); gu_repair<2>(img); gu_repair<3>(img); gu_repair<4>(img);
        gu_repair<5>(img); gu_repair<6>(img); gu_repair<7>(img); gu_repair<8>(img);
        lds_claim(L_CTL + 12);
        waitvm<PTHR>(); ISSUE_Q(2, s.st2, s.seq2);
        if (wave == 1) { IMG_DONE(); waitvm<PTHR>(); tile_dma(vk, vv, rk, rv, kv0 + (T_LDS + 13) * 4096, L_TILES + 13 * 8192); s.ops += 8; }
      }
#undef PARK
    } else {
      s.seq0 = s.seq1 = s.seq2 = s.ops - 64;
      s.st0 = s.st1 = s.st2 = LIN_KEEP ? 5 : 1;
    }
    if (wave != 0) {
      unsigned spins = 0;
      while (ctl[2] != (unsigned)(layer + 1) && spin_more<1>(spins)) {}
      asm volatile("" ::: "memory");
    }
    a.acc = __builtin_bit_cast(f32x4, zero4()); a.m = -INFINITY; a.l = asf(zero_u());
    STAMP(wave == 0 ? 3 + 12 * layer : (wave == 1 && layer == 0 ? 40 : 31));

    int e = wave == 0 ? 0 : 2;
#define WAIT_SLOT(SEQ) do { const int yg_ = s.ops - SEQ; \
      if (yg_ == 16) waitvm<16>(); else if (yg_ >= 29) waitvm<29>(); else if (yg_ >= 21) waitvm<21>(); else if (yg_ >= 13) waitvm<13>(); \
      else if (yg_ >= 8) waitvm<8>(); else waitvm<0>(); } while (0)
#define STEP(S, ST, SEQ) do { if (ST != 0) { \
      if ((ST & 4) == 0) { const QFrag q_ = q_frag<false>(qar); WAIT_SLOT(SEQ); tile_compute<S, false>(a, q_); } \
      bool lin_ = (ST & 4) != 0, lds_ = (LDSALL || S == 1) && s.nl != 0 && e >= LDSLAP; \
      if (lin_) WAIT_SLOT(SEQ); \
      while (lin_ || lds_) { \
        const QFrag q_ = q_frag<true>(qal); \
        if (!lin_) { tile_from_lds<S>(vv + s.ldsa); s.ldsa += 4 * 8192; --s.nl; lds_ = false; } \
        lin_ = false; \
        tile_compute<S, true>(a, q_); \
      } \
      if (s.next < nstream) { ISSUE_Q(S, ST, SEQ); PROG(); } \
      else if ((ST & 2) == 0 && layer == 0) { \
        tile_load<S, P_NT, LIN_KEEP>(LIN_KEEP ? vk : vkr, vv, rk, rv, kv0 + (T_KEEP + 4 * S + wave) * 4096); s.ops += 8; SEQ = s.ops; ST = LIN_KEEP ? 7 : 3; \
        if (wave != 0 && (s.st0 & s.st1 & s.st2 & 2) != 0) { IMG_DONE(); wowd_dma(cu, mu, wp, vv); s.ops += 13; } } \
      else { ST = 0; } \
    } } while (0)
#define PROG() do {} while (0)
    for (;;) {
      STEP(0, s.st0, s.seq0);
      STEP(1, s.st1, s.seq1);
      STEP(2, s.st2, s.seq2);
      for (int r = 0; r < XPL; ++r, ++e) {
        if (e >= 11) continue;
        const QFrag q = q_frag<true>(qal);
        switch (e) {
          case 0: tile_compute<3, true>(a, q); break;
          case 1: tile_compute<4, true>(a, q); break;
          case 2: tile_compute<5, true>(a, q); break;
          case 3: tile_compute<6, true>(a, q); break;
          case 4: tile_compute<7, true>(a, q); break;
          case 5: tile_compute<8, true>(a, q); break;
          case 6: tile_compute<9, true>(a, q); break;
          case 7: tile_compute<10, true>(a, q); break;
          case 8: tile_compute<11, true>(a, q); break;
          case 9: tile_compute<12, true>(a, q); break;
          default: tile_compute<13, true>(a, q); break;
        }
      }
      if (e >= 11 && (s.st0 | s.st1 | s.st2) == 0) break;
    }
#undef IMG_DONE
#undef STEP
#undef WAIT_SLOT
#undef PROG
#undef ISSUE_Q
    STAMP(wave == 0 ? 4 + 12 * layer : 27 + wave);

    {
      REFRESH();
      LAS float* mrg = (LAS float*)(lds + L_MRG) + 260 * wave;
      const float lsum = xrow_sum(a.l);
      const int h = (lane >> 3) & 1;
#pragma unroll
      for (int r = 0; r < 4; ++r) mrg[128 * h + 32 * (lane >> 4) + 8 * r + (lane & 7)] = a.acc[r];
      if ((lane & 55) == 0) { mrg[256 + 2 * h] = a.m; mrg[257 + 2 * h] = lsum; }
    }
    BAR();
    REFRESH();
    {
      LAS float* mrg = (LAS float*)(lds + L_MRG);
      const int tid = 64 * wave + lane, h = wave >> 1;
      float mw[4], M = -INFINITY;
#pragma unroll
      for (int w = 0; w < 4; ++w) { mw[w] = mrg[260 * w + 256 + 2 * h]; M = fmaxf(M, mw[w]); }
      float o = 0.f, L = 0.f;
#pragma unroll
      for (int w = 0; w < 4; ++w) { const float ew = __builtin_amdgcn_exp2f(mw[w] - M); o = fmaf(ew, mrg[260 * w + tid], o); L = fmaf(ew, mrg[260 * w + 257 + 2 * h], L); }
      __builtin_amdgcn_raw_buffer_store_b64(v2u{asu(o), tg + 1}, rw, rec + R_O + 8 * tid, 0, AUX_SC1);
      if ((tid & 127) == 0) __builtin_amdgcn_raw_buffer_store_b128(v4u{asu(M), tg + 1, asu(L), tg + 1}, rw, rec + R_ML + 16 * h, 0, AUX_SC1);
    }
    STAMP(wave == 0 ? 5 + 12 * layer : 31);
    if (wave == 0) {
      v4u g0 = zero4(), g1 = zero4(), ml = zero4();
      const int off = W_REC + (16 * grp + (lane >> 2)) * REC;
      unsigned spins = 0;
      bool ok = false;
      do {
        if (!ok) {
          g0 = __builtin_amdgcn_raw_buffer_load_b128(rw, off + R_O + 128 * sp + 32 * (lane & 3), 0, AUX_SC1);
          g1 = __builtin_amdgcn_raw_buffer_load_b128(rw, off + R_O + 128 * sp + 32 * (lane & 3) + 16, 0, AUX_SC1);
          ml = __builtin_amdgcn_raw_buffer_load_b128(rw, off + R_ML + 16 * (sp >> 3), 0, AUX_SC1);
          ok = (g0[1] == tg + 1) & (g0[3] == tg + 1) & (g1[1] == tg + 1) & (g1[3] == tg + 1) & (ml[1] == tg + 1) & (ml[3] == tg + 1);
        }
      } while (!__all(ok) && spin_more<SL0>(spins));
      float M = asf(ml[0]);
      M = fmaxf(M, dpp<ROR4>(M)); M = fmaxf(M, dpp<ROR8>(M)); M = xrow_max(M);
      const float w = __builtin_amdgcn_exp2f(asf(ml[0]) - M);
      float v[5] = {w * asf(g0[0]), w * asf(g0[2]), w * asf(g1[0]), w * asf(g1[2]), w * asf(ml[2])};
#pragma unroll
      for (int k = 0; k < 5; ++k) { float t = v[k]; t += dpp<ROR4>(t); t += dpp<ROR8>(t); v[k] = xrow_sum(t); }
      const float il = __builtin_amdgcn_rcpf(v[4]);
      if (lane < 4) *(LAS v2u*)(lds + L_O + 8 * lane) = v2u{f2bf(v[0] * il) | (f2bf(v[1] * il) << 16), f2bf(v[2] * il) | (f2bf(v[3] * il) << 16)};
    } else {
      waitvm<0>();
    }
    BAR();
    REFRESH();
    STAMP(wave == 0 ? 6 + 12 * layer : 31);
    {
      const u64 t0 = __builtin_amdgcn_s_memrealtime();
      if (wave != 0) rows_gemv_publish<2, 8>(lds, L_WO, L_O, wp, lane, rw, rec + R_Y, tg + 2);
      if (isred) {
        pause_until(t0, DLY_Y1);
        gather_y<SL_Y>(lds, rw, rho, wave, lane, tg + 2);
        BAR();
        if (wave == 0) __builtin_amdgcn_raw_buffer_store_b64(v2u{finish_x(lds, lane), tg + 3}, rw, W_X + (lane >> 3) * XREP + 64 * rho + 8 * (lane & 7), 0, AUX_SC1);
        STAMP(wave == 0 ? 7 + 12 * layer : 31);
      }
      if (wave == 0) {
        REFRESH();
        v2u xp[3];
        pause_until(t0, DLY_X1);
        gather_x<SL_X>(rw, cu, lane, tg + 3, xp);
        STAMP(8 + 12 * layer);
        layernorm_to_lds(xp, lds, lane);
      }
    }
    BAR();
    REFRESH();
    STAMP(wave == 0 ? 9 + 12 * layer : 31);
    if (wave != 0) {
      float acc[6];
#pragma unroll
      for (int k = 0; k < 6; ++k) acc[k] = asf(zero_u());
      LAS unsigned* ln2 = (LAS unsigned*)(lds + L_LN) + 9 * (lane & 31);
      unsigned in2[9];
#pragma unroll
      for (int i = 0; i < 9; ++i) in2[i] = ln2[i];
      gu_pair<0>(acc, in2[0]); gu_pair<1>(acc, in2[1]); gu_pair<2>(acc, in2[2]); gu_pair<3>(acc, in2[3]); gu_pair<4>(acc, in2[4]);
      gu_pair<5>(acc, in2[5]); gu_pair<6>(acc, in2[6]); gu_pair<7>(acc, in2[7]); gu_pair<8>(acc, in2[8]);
      asm volatile("s_nop 2" : "+v"(acc[0]), "+v"(acc[1]), "+v"(acc[2]), "+v"(acc[3]), "+v"(acc[4]), "+v"(acc[5]));
      unsigned hb[3];
#pragma unroll
      for (int k = 0; k < 3; ++k) {
        float t = swap16_add(acc[k], acc[k + 3]);
        t += dpp<XOR1>(t); t += dpp<XOR2>(t); t += dpp<ROR4>(t); t += dpp<ROR8>(t);
        auto sw = __builtin_amdgcn_permlane32_swap(asu(t), asu(t), false, false);
        const float g = rbf(asf(sw[0])), u = rbf(asf(sw[1]));
        hb[k] = f2bf((g > 0.f ? g : g * 0.f) * u);
      }
      if ((lane & 47) == 0) {
        LAS u16* h = (LAS u16*)(lds + L_H) + 6 * wp + 3 * (lane >> 4);
        h[0] = (u16)hb[0]; h[1] = (u16)hb[1]; h[2] = (u16)hb[2];
      }
    }
    BAR();
    REFRESH();
    STAMP(wave == 0 ? 10 + 12 * layer : 31);
    {
      const u64 t0 = __builtin_amdgcn_s_memrealtime();
      if (wave != 0) rows_gemv_publish<3, 9>(lds, L_WD, L_H, wp, lane, rw, rec + R_Y, tg + 4);
      if (isred) {
        pause_until(t0, DLY_Y2);
        gather_y<SL_Y>(lds, rw, rho, wave, lane, tg + 4);
        BAR();
        if (wave == 0) {
          const unsigned xn = finish_x(lds, lane);
          if (layer == 0) {
            __builtin_amdgcn_raw_buffer_store_b64(v2u{xn, 5u}, rw, W_X + (lane >> 3) * XREP + 64 * rho + 8 * (lane & 7), 0, AUX_SC1);
          } else {
            const __amdgpu_buffer_rsrc_t ro = __builtin_amdgcn_make_buffer_rsrc((void*)kargs()->out, 0, D * 2, 0x00020000);
            if (lane < 8) __builtin_amdgcn_raw_buffer_store_b32(xn, ro, 2 * piece_col(4 * rho + (lane >> 1)) + 4 * (lane & 1), 0, 0);
            if (cu == 0 && lane == 0) { const u64 en = (((u64)ctl[5] << 32) | ctl[4]) + 2; __builtin_amdgcn_raw_buffer_store_b64(v2u{(unsigned)en, (unsigned)(en >> 32)}, rw, W_EPOCH, 0, AUX_SC1); }
          }
        }
        STAMP(wave == 0 ? 11 + 12 * layer : 31);
      }
      if (wave == 0 && layer == 0) {
        REFRESH();
        v2u xp[3];
        pause_until(t0, DLY_X2);
        gather_x<SL_X>(rw, cu, lane, 5, xp);
        STAMP(12);
        layernorm_to_lds(xp, lds, lane);
      }
    }
  }
  asm volatile("" :: "a"(pin0), "a"(pin1));
}

#undef REFRESH
#undef grp
#undef sp
#undef wp
#undef mu
#undef kvb
#undef rec
#undef rho
#undef isred

}

extern "C" void kernel_launch(void* const* d_in, const int* in_sizes, int n_in, void* d_out, int out_size, void* d_ws, size_t ws_size,
                              hipStream_t stream) {
  Params p;
  p.x = (const u16*)d_in[0]; p.K = (const u16*)d_in[1]; p.V = (const u16*)d_in[2]; p.wq = (const u16*)d_in[3]; p.wo = (const u16*)d_in[4];
  p.wg = (const u16*)d_in[6]; p.wu = (const u16*)d_in[7]; p.wd = (const u16*)d_in[8];
  p.out = (u16*)d_out; p.ws = (unsigned char*)d_ws;
  (void)hipFuncSetAttribute((const void*)fused_decode, hipFuncAttributeMaxDynamicSharedMemorySize, LDS_BYTES);
  hipLaunchKernelGGL(fused_decode, dim3(NCU), dim3(256), LDS_BYTES, stream, p);
}
```

```cpp
#include <hip/hip_runtime.h>
#include <hip/hip_bf16.h>
#include <cstdint>

#define DIAG 0

namespace {

typedef unsigned v4u __attribute__((ext_vector_type(4)));
typedef unsigned v2u __attribute__((ext_vector_type(2)));
typedef float f32x4 __attribute__((ext_vector_type(4)));
typedef float f32x16 __attribute__((ext_vector_type(16)));
typedef float f32x32 __attribute__((ext_vector_type(32)));
typedef __bf16 bf16x2 __attribute__((ext_vector_type(2)));
typedef unsigned short u16;
typedef unsigned long long u64;
#define LAS __attribute__((address_space(3)))
#define GAS __attribute__((address_space(1)))

constexpr int D = 576, DH = 128, F = 4608, QD = 4096;
constexpr int NCU = 256;
constexpr float C2 = 0.088388347648318440f * 1.4426950408889634f;

constexpr int NLDS = 14;
constexpr int T_W0 = 36;
constexpr int T_LDS = 38;
constexpr int T_KEEP = T_LDS + NLDS;
constexpr int T_Q = T_KEEP + 12;
constexpr int NQ = 256 - T_Q;
#define NL2TILES 16
constexpr int NL2 = NL2TILES;
#define L2POS 3
constexpr int NPRE = L2POS;
#define ROWS3 4
constexpr int NROW3 = ROWS3, NROW4 = (NQ - 3 * NROW3) / 4;
static_assert(3 * NROW3 + 4 * NROW4 == NQ && NL2 % 4 == 0 && NQ % 4 == 0, "tile lists");

constexpr int L_TILES = 0;
constexpr int L_WO = NLDS * 8192;
constexpr int L_WD = L_WO + 16 * D * 2;
constexpr int L_MRG = L_WD + 18 * D * 2;
constexpr int L_LN = L_MRG + 4 * 260 * 4;
constexpr int L_Q = L_LN + D * 2;
constexpr int L_O = L_Q + 512;
constexpr int L_H = L_O + 64;
constexpr int L_QP = L_H + 64;
constexpr int L_CTL = L_QP + 256;
constexpr int L_END = L_CTL + 128;
constexpr int L_IMG = L_WO - 2 * 8192;
static_assert(L_IMG + 2 * D * 48 <= L_MRG, "gate/up image");
constexpr int LDS_BYTES = 163840;
static_assert(L_END <= LDS_BYTES, "LDS budget");

constexpr int W_EPOCH = 0;
constexpr int NRED = 36, NREP = 8, XREP = 2560;
constexpr int W_X = 4096;
constexpr int W_FLAG = 28672;
constexpr int W_Q = 32768;
constexpr int W_REC = 65536, REC = 7168;
constexpr int R_O = 0, R_Y = 2048, R_ML = R_Y + 4608;
constexpr int W_DIAG = W_REC + NCU * REC;
constexpr int W_END = W_DIAG + NCU * 64 * 8;
constexpr int AUX_SC1 = 16;

#define LDSLAP 8
#define LDSALL 0
#define XPL 1
#define MPER 0
constexpr u64 mall_rows() { u64 m = 0; if (MPER > 0) for (int r = 16; r < 64; ++r) if (r % (MPER > 0 ? MPER : 1) == 0) m |= 1ull << r; return m; }
constexpr u64 MALLROWS = mall_rows();
#define PTHR 8
#define LIN_L2 true
#define L2PER 3
constexpr int L2MUL = 256 / L2PER + 1;
#define LIN_KEEP false
#define LIN_HEAD 0
#define LEVEL_STR 1
constexpr unsigned STRADDLE = 0x11224488u;
#define GUW 14
#define SL0 2
#define SL_Y 1
#define SL_X 1
#define SL_Q 1
#define DLY_Q 40
#define DLY_Y1 90
#define DLY_X1 220
#define DLY_Y2 90
#define DLY_X2 220

struct Params {
  const u16 *x, *K, *V, *wq, *wo, *wg, *wu, *wd;
  u16* out;
  unsigned char* ws;
};

__device__ __forceinline__ float rbf(float f) { return (float)(__bf16)f; }
__device__ __forceinline__ unsigned f2bf(float f) { return (unsigned)__builtin_bit_cast(u16, (__bf16)f); }
__device__ __forceinline__ float bf2f(unsigned b) { return __builtin_bit_cast(float, b << 16); }
__device__ __forceinline__ float asf(unsigned u) { return __builtin_bit_cast(float, u); }
__device__ __forceinline__ unsigned asu(float f) { return __builtin_bit_cast(unsigned, f); }
__device__ __forceinline__ float dot2(unsigned a, unsigned b, float c) {
  return __builtin_amdgcn_fdot2_f32_bf16(__builtin_bit_cast(bf16x2, a), __builtin_bit_cast(bf16x2, b), c, false);
}
template <int CTRL> __device__ __forceinline__ float dpp(float x) {
  return asf((unsigned)__builtin_amdgcn_mov_dpp((int)asu(x), CTRL, 0xf, 0xf, true));
}
constexpr int XOR1 = 0xB1, XOR2 = 0x4E, ROR4 = 0x124, ROR8 = 0x128;
__device__ __forceinline__ float swap16_add(float a, float b) {
  auto s = __builtin_amdgcn_permlane16_swap(asu(a), asu(b), false, false);
  return asf(s[0]) + asf(s[1]);
}
__device__ __forceinline__ float swap32_add(float a, float b) {
  auto s = __builtin_amdgcn_permlane32_swap(asu(a), asu(b), false, false);
  return asf(s[0]) + asf(s[1]);
}
__device__ __forceinline__ float xrow_max(float x) {
  auto s = __builtin_amdgcn_permlane16_swap(asu(x), asu(x), false, false);
  x = fmaxf(asf(s[0]), asf(s[1]));
  auto t = __builtin_amdgcn_permlane32_swap(asu(x), asu(x), false, false);
  return fmaxf(asf(t[0]), asf(t[1]));
}
__device__ __forceinline__ float xrow_sum(float x) { x = swap16_add(x, x); return swap32_add(x, x); }
__device__ __forceinline__ float wave_sum(float x) {
  x += dpp<XOR1>(x); x += dpp<XOR2>(x); x += dpp<ROR4>(x); x += dpp<ROR8>(x);
  return xrow_sum(x);
}

__device__ __forceinline__ int fresh_lane() { int l; asm volatile("v_mbcnt_lo_u32_b32 %0, -1, 0\n\tv_mbcnt_hi_u32_b32 %0, -1, %0" : "=v"(l)); return l; }
__device__ __forceinline__ unsigned zero_u() { unsigned z; asm volatile("v_mov_b32 %0, 0" : "=v"(z)); return z; }
__device__ __forceinline__ v4u zero4() { const unsigned z = zero_u(); return v4u{z, z, z, z}; }
__device__ __forceinline__ int fresh(int x) { asm volatile("" : "+s"(x)); return x; }
__device__ __forceinline__ const Params* kargs() {
  auto k = (const __attribute__((address_space(4))) Params*)__builtin_amdgcn_kernarg_segment_ptr();
  asm volatile("" : "+s"(k));
  return (const Params*)k;
}
#define BAR() asm volatile("s_waitcnt lgkmcnt(0)\n\ts_barrier" ::: "memory")
template <int N> __device__ __forceinline__ void waitvm() { asm volatile("s_waitcnt vmcnt(%c0)" :: "i"(N) : "memory"); }

template <int S> struct Slot { static constexpr bool acc = S >= 6; static constexpr int base = S >= 6 ? 32 * (S - 6) : 64 + 32 * S; };

#define LD1(P, R, VOFF, RS, OFF, POL) "buffer_load_dwordx4 " P "[%c0+" #R ":%c0+" #R "+3], " VOFF ", " RS ", %5 offen offset:" #OFF POL "\n\t"
#define LDV(P, POL) LD1(P, 16, "%2", "%4", 0, POL) LD1(P, 20, "%2", "%4", 1024, POL) LD1(P, 24, "%2", "%4", 2048, POL) LD1(P, 28, "%2", "%4", 3072, POL)
#define LD8R(P, POL) LD1(P, 0, "%1", "%3", 0, POL) LD1(P, 4, "%1", "%3", 64, POL) LD1(P, 8, "%1", "%3", 128, POL) LD1(P, 12, "%1", "%3", 192, POL) LDV(P, POL)
#define LD8L(P, POL) LD1(P, 0, "%1", "%3", 0, POL) LD1(P, 4, "%1", "%3", 1024, POL) LD1(P, 8, "%1", "%3", 2048, POL) LD1(P, 12, "%1", "%3", 3072, POL) LDV(P, POL)
#define LDOPS :: "i"(Slot<S>::base), "v"(vk), "v"(vv), "s"(rk), "s"(rv), "s"(soff) : "memory"
enum { P_KEEP = 0, P_NT = 1 };
template <int S, int POL, bool LIN>
__device__ __forceinline__ void tile_load(int vk, int vv, __amdgpu_buffer_rsrc_t rk, __amdgpu_buffer_rsrc_t rv, int soff) {
  if constexpr (Slot<S>::acc) {
    static_assert(POL == P_NT && LIN, "resident slots");
    asm volatile(LD8L("a", " nt") LDOPS);
  } else if constexpr (LIN) {
    static_assert(POL == P_NT, "linear tiles stream");
    asm volatile(LD8L("v", " nt") LDOPS);
  } else {
    if constexpr (POL == P_NT) asm volatile(LD8R("v", " nt") LDOPS); else asm volatile(LD8R("v", "") LDOPS);
  }
}
__device__ __forceinline__ int k_off_rows(int lane) { return (4 * (lane & 3) + ((lane >> 2) & 3)) * 256 + (lane >> 4) * 16; }
__device__ __forceinline__ int k_off_lin(int lane) { return ((lane >> 2) & 3) * 256 + (lane >> 4) * 64 + (lane & 3) * 16; }
__device__ __forceinline__ void dma1k(int voff, __amdgpu_buffer_rsrc_t rs, int soff, int m0v) {
  asm volatile("s_mov_b32 m0, %3\n\ts_nop 0\n\tbuffer_load_dwordx4 %0, %1, %2 offen nt lds" :: "v"(voff), "s"(rs), "s"(soff), "s"(m0v) : "memory");
}
__device__ __forceinline__ void tile_dma(int vk, int vv, __amdgpu_buffer_rsrc_t rk, __amdgpu_buffer_rsrc_t rv, int soff, int ldsb) {
#pragma unroll
  for (int i = 0; i < 4; ++i) dma1k(vk, rk, soff + 1024 * i, ldsb + 1024 * i);
#pragma unroll
  for (int i = 0; i < 4; ++i) dma1k(vv, rv, soff + 1024 * i, ldsb + 4096 + 1024 * i);
}
#define DR1(R, OFF) "ds_read_b128 v[%c0+" #R ":%c0+" #R "+3], %1 offset:" #OFF "\n\t"
template <int S> __device__ __forceinline__ void tile_from_lds(int addr) {
  static_assert(!Slot<S>::acc, "streaming slots only");
  asm volatile(DR1(0, 0) DR1(4, 1024) DR1(8, 2048) DR1(12, 3072) DR1(16, 4096) DR1(20, 5120) DR1(24, 6144) DR1(28, 7168) "s_waitcnt lgkmcnt(0)"
               :: "i"(Slot<S>::base), "v"(addr) : "memory");
}

struct Att { unsigned k0, k1, k2, k3; f32x4 acc; float m, l; };
struct QFrag { v4u q0, q1, q2, q3; };
template <bool LIN> __device__ __forceinline__ QFrag q_frag(int addr) {
  QFrag q;
  LAS v4u* p = (LAS v4u*)(uintptr_t)(unsigned)addr;
  q.q0 = p[0];
  if constexpr (!LIN) { q.q1 = p[4]; q.q2 = p[8]; q.q3 = p[12]; }
  return q;
}

#define QK4(P) "s_nop 1\n\tv_mfma_f32_16x16x32_bf16 %0, " P "[%c5:%c5+3], %1, 0\n\t" \
               "v_mfma_f32_16x16x32_bf16 %0, " P "[%c5+4:%c5+7], %2, %0\n\t" \
               "v_mfma_f32_16x16x32_bf16 %0, " P "[%c5+8:%c5+11], %3, %0\n\t" \
               "v_mfma_f32_16x16x32_bf16 %0, " P "[%c5+12:%c5+15], %4, %0\n\ts_nop 7"
#define QK4I(P) "s_nop 1\n\tv_mfma_f32_16x16x32_bf16 %0, " P "[%c5:%c5+3], %4, 0\n\t" \
                "v_mfma_f32_16x16x32_bf16 %1, " P "[%c5+4:%c5+7], %4, 0\n\t" \
                "v_mfma_f32_16x16x32_bf16 %2, " P "[%c5+8:%c5+11], %4, 0\n\t" \
                "v_mfma_f32_16x16x32_bf16 %3, " P "[%c5+12:%c5+15], %4, 0\n\ts_nop 7"
#define PV1(P) "s_nop 1\n\tv_mfma_f32_16x16x32_bf16 %0, " P "[%c2:%c2+3], %1, %0\n\ts_nop %c3"
template <int S, bool LIN> __device__ __forceinline__ void tile_compute(Att& a, const QFrag& q) {
  f32x4 s;
  if constexpr (LIN) {
    f32x4 d0, d1, d2, d3;
    if constexpr (Slot<S>::acc) asm volatile(QK4I("a") : "=&v"(d0), "=&v"(d1), "=&v"(d2), "=&v"(d3) : "v"(q.q0), "i"(Slot<S>::base));
    else asm volatile(QK4I("v") : "=&v"(d0), "=&v"(d1), "=&v"(d2), "=&v"(d3) : "v"(q.q0), "i"(Slot<S>::base));
    s[0] = (dpp<0x00>(d0[0]) + dpp<0x55>(d0[1])) + (dpp<0xAA>(d0[2]) + dpp<0xFF>(d0[3]));
    s[1] = (dpp<0x00>(d1[0]) + dpp<0x55>(d1[1])) + (dpp<0xAA>(d1[2]) + dpp<0xFF>(d1[3]));
    s[2] = (dpp<0x00>(d2[0]) + dpp<0x55>(d2[1])) + (dpp<0xAA>(d2[2]) + dpp<0xFF>(d2[3]));
    s[3] = (dpp<0x00>(d3[0]) + dpp<0x55>(d3[1])) + (dpp<0xAA>(d3[2]) + dpp<0xFF>(d3[3]));
  } else {
    static_assert(LIN || !Slot<S>::acc, "resident slots are linear");
    asm volatile(QK4("v") : "=&v"(s) : "v"(q.q0), "v"(q.q1), "v"(q.q2), "v"(q.q3), "i"(Slot<S>::base));
  }
  const float mx = xrow_max(fmaxf(fmaxf(s[0], s[1]), fmaxf(s[2], s[3])));
  const float mn = fmaxf(a.m, mx * C2);
  const float alpha = __builtin_amdgcn_exp2f(a.m - mn);
  a.m = mn;
  a.acc *= alpha;
  float ps = 0.f;
#pragma unroll
  for (int t = 0; t < 4; ++t) {
    const float p = __builtin_amdgcn_exp2f(fmaf(s[t], C2, -mn));
    ps += p;
    bf16x2 pb = {(__bf16)p, (__bf16)p};
    const unsigned pp = __builtin_bit_cast(unsigned, pb);
    const v4u b = {pp & a.k0, pp & a.k1, pp & a.k2, pp & a.k3};
    if constexpr (Slot<S>::acc) asm volatile(PV1("a") : "+v"(a.acc) : "v"(b), "i"(Slot<S>::base + 16 + 4 * t), "i"(t == 3 ? 7 : 0));
    else asm volatile(PV1("v") : "+v"(a.acc) : "v"(b), "i"(Slot<S>::base + 16 + 4 * t), "i"(t == 3 ? 7 : 0));
  }
  a.l = fmaf(a.l, alpha, ps);
}

__device__ __forceinline__ int lds_claim(int addr) {
  int t; unsigned tmp; const unsigned one = 1;
  asm volatile("s_mov_b64 exec, 1\n\tds_add_rtn_u32 %1, %2, %3\n\ts_waitcnt lgkmcnt(0)\n\tv_readfirstlane_b32 %0, %1\n\ts_mov_b64 exec, -1"
               : "=s"(t), "=&v"(tmp) : "v"(addr), "v"(one) : "memory");
  return t;
}
constexpr unsigned SEL_LO = 0x05040100u, SEL_HI = 0x07060302u;
__device__ __forceinline__ unsigned perm2(unsigned a, unsigned b, unsigned sel) { return __builtin_amdgcn_perm(b, a, sel); }

constexpr unsigned SPIN_CAP = 1u << 21;
template <int SLEEP> __device__ __forceinline__ bool spin_more(unsigned& n) {
  __builtin_amdgcn_s_sleep(SLEEP);
  n = (unsigned)fresh((int)n + 1);
  return n < SPIN_CAP;
}
__device__ __forceinline__ void pause_until(u64 t0, unsigned ticks) {
  while ((unsigned)(__builtin_amdgcn_s_memrealtime() - t0) < ticks) __builtin_amdgcn_s_sleep(1);
}

__device__ __forceinline__ int piece_col(int P) { return P < 96 ? 12 * (P >> 1) + 4 * (P & 1) : 12 * (P - 96) + 8; }

__device__ __forceinline__ void layernorm_to_lds(const v2u (&xp)[3], LAS unsigned char* lds, int lane) {
  const bool live = lane < 48;
  float x[12];
#pragma unroll
  for (int k = 0; k < 3; ++k) {
    x[4 * k] = asf(xp[k][0] << 16); x[4 * k + 1] = asf(xp[k][0] & 0xffff0000u); x[4 * k + 2] = asf(xp[k][1] << 16); x[4 * k + 3] = asf(xp[k][1] & 0xffff0000u);
  }
  float s = 0.f;
#pragma unroll
  for (int i = 0; i < 12; ++i) s += x[i];
  const float mean = wave_sum(live ? s : 0.f) * (1.f / D);
  float v = 0.f;
#pragma unroll
  for (int i = 0; i < 12; ++i) { const float d = x[i] - mean; v = fmaf(d, d, v); }
  const float var = rbf(wave_sum(live ? v : 0.f) * (1.f / D));
  const float mb = rbf(mean), inv = __builtin_amdgcn_rcpf(rbf(__builtin_amdgcn_sqrtf(rbf(var + 1e-5f))));
  if (live) {
#pragma unroll
    for (int k = 0; k < 3; ++k) {
      bf16x2 lo = {(__bf16)(rbf(x[4 * k] - mb) * inv), (__bf16)(rbf(x[4 * k + 1] - mb) * inv)};
      bf16x2 hi = {(__bf16)(rbf(x[4 * k + 2] - mb) * inv), (__bf16)(rbf(x[4 * k + 3] - mb) * inv)};
      *(LAS v2u*)(lds + L_LN + 2 * piece_col(lane + 48 * k)) = v2u{__builtin_bit_cast(unsigned, lo), __builtin_bit_cast(unsigned, hi)};
    }
  }
}

template <int NP, int NROWPAIRS>
__device__ __forceinline__ void rows_gemv_publish(LAS unsigned char* lds, int wbase, int inbase, int wp, int lane,
                                                  __amdgpu_buffer_rsrc_t rw, int ybase, unsigned tag) {
  const int rg = lane >> 4, c = lane & 15;
  float acc[12];
#pragma unroll
  for (int k = 0; k < 12; ++k) acc[k] = asf(zero_u());
#pragma unroll
  for (int i = 0; i < NP; ++i) {
    const int p = rg + 4 * i, pw = p < NROWPAIRS ? p : NROWPAIRS - 1;
    const unsigned in2 = *(LAS unsigned*)(lds + inbase + 4 * p);
    LAS v2u* r0 = (LAS v2u*)(lds + wbase + (2 * pw) * (D * 2) + 384 * wp + 24 * c);
    LAS v2u* r1 = (LAS v2u*)(lds + wbase + (2 * pw + 1) * (D * 2) + 384 * wp + 24 * c);
#pragma unroll
    for (int j = 0; j < 3; ++j) {
      const v2u a = r0[j], b = r1[j];
      acc[4 * j + 0] = dot2(perm2(a[0], b[0], SEL_LO), in2, acc[4 * j + 0]);
      acc[4 * j + 1] = dot2(perm2(a[0], b[0], SEL_HI), in2, acc[4 * j + 1]);
      acc[4 * j + 2] = dot2(perm2(a[1], b[1], SEL_LO), in2, acc[4 * j + 2]);
      acc[4 * j + 3] = dot2(perm2(a[1], b[1], SEL_HI), in2, acc[4 * j + 3]);
    }
  }
  const float y0 = swap32_add(swap16_add(acc[0], acc[2]), swap16_add(acc[4], acc[6]));
  const float y1 = swap32_add(swap16_add(acc[1], acc[3]), swap16_add(acc[5], acc[7]));
  const float y2 = swap32_add(swap16_add(acc[8], acc[9]), swap16_add(acc[10], acc[11]));
  const int idx = 64 * wp + 4 * c + rg;
  __builtin_amdgcn_raw_buffer_store_b128(v4u{asu(y0), tag, asu(y1), tag}, rw, ybase + 16 * idx, 0, AUX_SC1);
  __builtin_amdgcn_raw_buffer_store_b64(v2u{asu(y2), tag}, rw, ybase + 3072 + 8 * idx, 0, AUX_SC1);
}

template <int SLEEP>
__device__ __forceinline__ void gather_y(LAS unsigned char* lds, __amdgpu_buffer_rsrc_t rw, int rho, int wave, int lane, unsigned tag) {
  unsigned spins = 0;
  const int off = W_REC + (64 * wave + (lane >> 3)) * REC + R_Y + 128 * rho + 16 * (lane & 7);
  float s0, s1;
  bool ok;
  do {
    v4u a[8];
#pragma unroll
    for (int k = 0; k < 8; ++k) a[k] = __builtin_amdgcn_raw_buffer_load_b128(rw, off, k * 8 * REC, AUX_SC1);
    ok = true; s0 = 0.f; s1 = 0.f;
#pragma unroll
    for (int k = 0; k < 8; ++k) { ok &= (a[k][1] == tag) & (a[k][3] == tag); s0 += asf(a[k][0]); s1 += asf(a[k][2]); }
  } while (!__all(ok) && spin_more<SLEEP>(spins));
  s0 += dpp<ROR8>(s0); s1 += dpp<ROR8>(s1);
  s0 = xrow_sum(s0); s1 = xrow_sum(s1);
  if (lane < 8) *(LAS v2u*)(lds + L_QP + 64 * wave + 8 * lane) = v2u{asu(s0), asu(s1)};
}
__device__ __forceinline__ unsigned finish_x(LAS unsigned char* lds, int lane) {
  const int j = lane & 7;
  float s0 = 0.f, s1 = 0.f;
#pragma unroll
  for (int w = 0; w < 4; ++w) { const v2u t = *(LAS v2u*)(lds + L_QP + 64 * w + 8 * j); s0 += asf(t[0]); s1 += asf(t[1]); }
  LAS unsigned* res = (LAS unsigned*)(lds + L_CTL + 64) + j;
  const unsigned r = *res;
  bf16x2 xb = {(__bf16)(asf(r << 16) + rbf(s0)), (__bf16)(asf(r & 0xffff0000u) + rbf(s1))};
  const unsigned xn = __builtin_bit_cast(unsigned, xb);
  asm volatile("s_waitcnt lgkmcnt(0)" ::: "memory");
  *res = xn;
  return xn;
}
template <int SLEEP>
__device__ __forceinline__ void gather_x(__amdgpu_buffer_rsrc_t rw, int cu, int lane, unsigned tag, v2u (&xp)[3]) {
  unsigned spins = 0;
  v4u g[3] = {zero4(), zero4(), zero4()};
  const int off = W_X + (cu & (NREP - 1)) * XREP + 16 * (lane < 48 ? lane : 47);
  bool ok = false;
  do {
    if (!ok) {
#pragma unroll
      for (int k = 0; k < 3; ++k) g[k] = __builtin_amdgcn_raw_buffer_load_b128(rw, off + 768 * k, 0, AUX_SC1);
      ok = (g[0][1] == tag) & (g[0][3] == tag) & (g[1][1] == tag) & (g[1][3] == tag) & (g[2][1] == tag) & (g[2][3] == tag);
    }
  } while (!__all(ok) && spin_more<SLEEP>(spins));
#pragma unroll
  for (int k = 0; k < 3; ++k) xp[k] = v2u{g[k][0], g[k][2]};
}

#define STAMP(i) do {} while (0)
#define ST1(i) do {} while (0)

template <int K> __device__ __forceinline__ void wq_load_one(int voff, __amdgpu_buffer_rsrc_t rs) {
  asm volatile("buffer_load_dwordx4 v[%c0:%c0+3], %1, %2, %3 offen" :: "i"(136 + 4 * K), "v"(voff), "s"(rs), "s"(K * QD * 2) : "memory");
}
__device__ __forceinline__ void dma1k_shared(int voff, __amdgpu_buffer_rsrc_t rs, int soff, int m0v) {
  asm volatile("s_mov_b32 m0, %3\n\ts_nop 0\n\tbuffer_load_dwordx4 %0, %1, %2 offen lds" :: "v"(voff), "s"(rs), "s"(soff), "s"(m0v) : "memory");
}
constexpr int wq_reg(int j) { return j < 10 ? 214 + j : 48 + (j - 10); }
template <int DST0, int DST1, int RA, int RB> __device__ __forceinline__ void pair_regs() {
  asm volatile("v_perm_b32 v%c0, v%c3, v%c2, %4\n\tv_perm_b32 v%c1, v%c3, v%c2, %5" :: "i"(DST0), "i"(DST1), "i"(RA), "i"(RB), "s"(SEL_LO), "s"(SEL_HI));
}
template <int DST> __device__ __forceinline__ void pair_vals(unsigned a, unsigned b) {
  asm volatile("v_perm_b32 v%c0, %2, %1, %3\n\tv_perm_b32 v[%c0+1], %2, %1, %4" :: "i"(DST), "v"(a), "v"(b), "s"(SEL_LO), "s"(SEL_HI));
}
template <int I> __device__ __forceinline__ void gu_repair(LAS unsigned* img) {
  const unsigned a0 = img[24 * I], a1 = img[24 * I + 1], a2 = img[24 * I + 2], b0 = img[24 * I + 12], b1 = img[24 * I + 13], b2 = img[24 * I + 14];
  pair_vals<160 + 6 * I>(a0, b0); pair_vals<162 + 6 * I>(a1, b1); pair_vals<164 + 6 * I>(a2, b2);
}
template <int I> __device__ __forceinline__ void wq_repair() {
  pair_regs<wq_reg(8 * I + 0), wq_reg(8 * I + 1), 136 + 8 * I + 0, 140 + 8 * I + 0>();
  pair_regs<wq_reg(8 * I + 2), wq_reg(8 * I + 3), 136 + 8 * I + 1, 140 + 8 * I + 1>();
  pair_regs<wq_reg(8 * I + 4), wq_reg(8 * I + 5), 136 + 8 * I + 2, 140 + 8 * I + 2>();
  pair_regs<wq_reg(8 * I + 6), wq_reg(8 * I + 7), 136 + 8 * I + 3, 140 + 8 * I + 3>();
}
template <int R0, int R1> __device__ __forceinline__ void dot_regs(float& a0, float& a1, unsigned in2) {
  asm volatile("v_dot2c_f32_bf16 %0, v%c2, %4\n\tv_dot2c_f32_bf16 %1, v%c3, %4" : "+v"(a0), "+v"(a1) : "i"(R0), "i"(R1), "v"(in2));
}
template <int I> __device__ __forceinline__ void gu_pair(float (&acc)[6], unsigned in2) {
  dot_regs<160 + 6 * I + 0, 160 + 6 * I + 1>(acc[0], acc[1], in2);
  dot_regs<160 + 6 * I + 2, 160 + 6 * I + 3>(acc[2], acc[3], in2);
  dot_regs<160 + 6 * I + 4, 160 + 6 * I + 5>(acc[4], acc[5], in2);
}
template <int I> __device__ __forceinline__ void wq_pair(float (&acc)[8], unsigned in2) {
  dot_regs<wq_reg(8 * I + 0), wq_reg(8 * I + 1)>(acc[0], acc[1], in2);
  dot_regs<wq_reg(8 * I + 2), wq_reg(8 * I + 3)>(acc[2], acc[3], in2);
  dot_regs<wq_reg(8 * I + 4), wq_reg(8 * I + 5)>(acc[4], acc[5], in2);
  dot_regs<wq_reg(8 * I + 6), wq_reg(8 * I + 7)>(acc[6], acc[7], in2);
}

__device__ __forceinline__ void wowd_dma(int cu, int mu, int wp, int vv) {
  const Params* pp = kargs();
  const __amdgpu_buffer_rsrc_t ro = __builtin_amdgcn_make_buffer_rsrc((void*)pp->wo, 0, QD * D * 2, 0x00020000);
  const __amdgpu_buffer_rsrc_t rd = __builtin_amdgcn_make_buffer_rsrc((void*)pp->wd, 0, F * D * 2, 0x00020000);
#pragma unroll
  for (int i = 0; i < 6; ++i) dma1k(vv, ro, cu * (16 * D * 2) + (6 * wp + i) * 1024, L_WO + (6 * wp + i) * 1024);
#pragma unroll
  for (int i = 0; i < 7; ++i) {
    const int pc = 7 * wp + i;
    if (pc < 20 || vv < 256) dma1k(vv, rd, mu * (18 * D * 2) + pc * 1024, L_WD + pc * 1024);
  }
}

struct Stream { int st0, st1, st2, seq0, seq1, seq2, ops, next, nl, ldsa; };

__global__ void __attribute__((amdgpu_num_vgpr(48))) __launch_bounds__(256, 1) fused_decode(Params p) {
  extern __shared__ __attribute__((aligned(16))) unsigned char lds_raw[];
  LAS unsigned char* lds = (LAS unsigned char*)lds_raw;
  asm volatile("" ::: "v255", "a255");
  f32x32 pin0; f32x16 pin1;
  asm volatile("" : "=a"(pin0), "=a"(pin1));
  int wave = __builtin_amdgcn_readfirstlane(threadIdx.x >> 6), cu = blockIdx.x, lane = fresh_lane();
#define REFRESH() do { lane = fresh_lane(); cu = fresh(cu); wave = fresh(wave); } while (0)
#define grp (cu >> 4)
#define sp (cu & 15)
#define wp (wave - 1)
#define mu ((cu & 7) * 32 + (cu >> 3))
#define kvb (cu << 20)
#define rec (W_REC + cu * REC)
#define rho ((cu * 9363) >> 16)
#define isred (cu == 7 * rho && cu < 7 * NRED)
  const __amdgpu_buffer_rsrc_t rk = __builtin_amdgcn_make_buffer_rsrc((void*)p.K, 0, 1 << 28, 0x00020000);
  const __amdgpu_buffer_rsrc_t rv = __builtin_amdgcn_make_buffer_rsrc((void*)p.V, 0, 1 << 28, 0x00020000);
  const __amdgpu_buffer_rsrc_t rw = __builtin_amdgcn_make_buffer_rsrc((void*)p.ws, 0, W_END, 0x00020000);
  volatile LAS unsigned* ctl = (volatile LAS unsigned*)(lds + L_CTL);
  STAMP(wave == 0 ? 0 : 31);

  if (wave == 0) {
    const v2u e = __builtin_amdgcn_raw_buffer_load_b64(rw, W_EPOCH, 0, AUX_SC1);
    v2u xp[3];
#pragma unroll
    for (int k = 0; k < 3; ++k) xp[k] = *(const GAS v2u*)(p.x + piece_col((lane < 48 ? lane : 47) + 48 * k));
    const unsigned xr = *(const GAS unsigned*)(p.x + piece_col(4 * (isred ? rho : 0) + ((lane & 7) >> 1)) + 2 * (lane & 1));
#pragma unroll
    for (int k = 0; k < 7; ++k) __builtin_amdgcn_raw_buffer_store_b128(zero4(), rw, rec + 1024 * k + 16 * lane, 0, AUX_SC1);
    if (isred) __builtin_amdgcn_raw_buffer_store_b64(v2u{0, 0}, rw, W_X + (lane >> 3) * XREP + 64 * rho + 8 * (lane & 7), 0, AUX_SC1);
    if (lane < 4) ctl[lane] = 0u;
    if (lane < 16) *(LAS unsigned*)(lds + L_H + 4 * lane) = 0u;
    if (lane < 8) ctl[16 + lane] = xr;
    if (lane == 0) { ctl[4] = e[0]; ctl[5] = e[1]; }
    layernorm_to_lds(xp, lds, lane);
    waitvm<0>();
    const u64 tag = ((((u64)e[1] << 32) | e[0]) + 1) << 16;
    if (lane == 0) __builtin_amdgcn_raw_buffer_store_b64(v2u{(unsigned)tag, (unsigned)(tag >> 32)}, rw, W_FLAG + 8 * cu, 0, AUX_SC1);
  } else {
    ST1(32);
    const int vv = lane * 16, vk = k_off_lin(lane);
    const __amdgpu_buffer_rsrc_t rq = __builtin_amdgcn_make_buffer_rsrc((void*)p.wq, 0, D * QD * 2, 0x00020000);
    const int vq = (192 * wp + 6 * (lane >> 1)) * (QD * 2) + 32 * mu + 16 * (lane & 1);
    tile_dma(vk, vv, rk, rv, kvb + (T_LDS + wave) * 4096, L_TILES + wave * 8192);
    wq_load_one<0>(vq, rq);
    tile_dma(vk, vv, rk, rv, kvb + (T_LDS + wave + 4) * 4096, L_TILES + (wave + 4) * 8192);
    wq_load_one<1>(vq, rq);
    ST1(33);
    waitvm<PTHR + 2>(); tile_dma(vk, vv, rk, rv, kvb + (T_LDS + wave + 8) * 4096, L_TILES + (wave + 8) * 8192);
    wq_load_one<2>(vq, rq);
    waitvm<PTHR + 2>(); tile_load<5, P_NT, true>(vk, vv, rk, rv, kvb + wave * 4096);
    wq_load_one<3>(vq, rq);
    waitvm<PTHR + 2>(); tile_load<6, P_NT, true>(vk, vv, rk, rv, kvb + (4 + wave) * 4096);
    wq_load_one<4>(vq, rq);
    waitvm<PTHR + 2>(); tile_load<7, P_NT, true>(vk, vv, rk, rv, kvb + (8 + wave) * 4096);
    wq_load_one<5>(vq, rq);
    waitvm<PTHR + 2>(); tile_load<8, P_NT, true>(vk, vv, rk, rv, kvb + (12 + wave) * 4096);
    ST1(34);
  }

  Stream s;
  s.ops = 0;

  for (int layer = 0; layer < 2; ++layer) {
    const unsigned tg = 4 * layer;
    BAR();
    REFRESH();
    if (wave != 0) {
      if (layer == 0) { waitvm<8>(); wq_repair<0>(); wq_repair<1>(); wq_repair<2>(); }
      float acc[8];
#pragma unroll
      for (int k = 0; k < 8; ++k) acc[k] = asf(zero_u());
      LAS unsigned* ln2 = (LAS unsigned*)(lds + L_LN) + 96 * wp + 3 * (lane >> 1);
      const unsigned i0 = ln2[0], i1 = ln2[1], i2 = ln2[2];
      wq_pair<0>(acc, i0); wq_pair<1>(acc, i1); wq_pair<2>(acc, i2);
      asm volatile("s_nop 2" : "+v"(acc[0]), "+v"(acc[1]), "+v"(acc[2]), "+v"(acc[3]), "+v"(acc[4]), "+v"(acc[5]), "+v"(acc[6]), "+v"(acc[7]));
      float u[2];
#pragma unroll
      for (int k = 0; k < 2; ++k) {
        float t = swap16_add(swap32_add(acc[k], acc[k + 4]), swap32_add(acc[k + 2], acc[k + 6]));
        t += dpp<ROR8>(t); t += dpp<ROR4>(t); t += dpp<XOR2>(t);
        u[k] = t;
      }
      if ((lane & 14) == 0) {
        LAS float* qp = (LAS float*)(lds + L_QP) + 16 * wp + 8 * (lane & 1) + 4 * (lane >> 5) + 2 * ((lane >> 4) & 1);
        qp[0] = u[0]; qp[1] = u[1];
      }
    }
    BAR();
    REFRESH();
    STAMP(wave == 0 ? 1 + 12 * layer : 31);
    if (wave == 0) {
      const u64 epoch = ((u64)(unsigned)__builtin_amdgcn_readfirstlane(ctl[5]) << 32) | (unsigned)__builtin_amdgcn_readfirstlane(ctl[4]);
      const u64 tag = (epoch + 1 + layer) << 16;
      if (lane < 16) {
        LAS float* qp = (LAS float*)(lds + L_QP) + lane;
        const u64 g = tag | f2bf((qp[0] + qp[16]) + qp[32]);
        __builtin_amdgcn_raw_buffer_store_b64(v2u{(unsigned)g, (unsigned)(g >> 32)}, rw, W_Q + mu * 128 + 8 * lane, 0, AUX_SC1);
      }
      const unsigned tlo = (unsigned)tag >> 16, thi = (unsigned)(tag >> 32);
      {
        v4u g0 = zero4(), g1 = zero4();
        const u64 t0 = __builtin_amdgcn_s_memrealtime();
        bool ok = false;
        unsigned spins = 0;
        pause_until(t0, DLY_Q);
        do {
          if (!ok) {
            g0 = __builtin_amdgcn_raw_buffer_load_b128(rw, W_Q + 2048 * grp + 32 * lane, 0, AUX_SC1);
            g1 = __builtin_amdgcn_raw_buffer_load_b128(rw, W_Q + 2048 * grp + 32 * lane + 16, 0, AUX_SC1);
            ok = ((g0[0] >> 16) == tlo) & (g0[1] == thi) & ((g0[2] >> 16) == tlo) & (g0[3] == thi) &
                 ((g1[0] >> 16) == tlo) & (g1[1] == thi) & ((g1[2] >> 16) == tlo) & (g1[3] == thi);
          }
        } while (!__all(ok) && spin_more<SL_Q>(spins));
        *(LAS v2u*)(lds + L_Q + 8 * lane) = v2u{perm2(g0[0], g0[2], SEL_LO), perm2(g1[0], g1[2], SEL_LO)};
        asm volatile("s_waitcnt lgkmcnt(0)" ::: "memory");
        ctl[2] = layer + 1;
        STAMP(layer == 0 ? 24 : 31);
      }
      if (layer == 0) {
        unsigned spins = 0;
        bool ok;
        do {
          const v4u f0 = __builtin_amdgcn_raw_buffer_load_b128(rw, W_FLAG + 16 * lane, 0, AUX_SC1);
          const v4u f1 = __builtin_amdgcn_raw_buffer_load_b128(rw, W_FLAG + 1024 + 16 * lane, 0, AUX_SC1);
          ok = (f0[0] == tlo << 16) & (f0[1] == thi) & (f0[2] == tlo << 16) & (f0[3] == thi) &
               (f1[0] == tlo << 16) & (f1[1] == thi) & (f1[2] == tlo << 16) & (f1[3] == thi);
        } while (!__all(ok) && spin_more<32>(spins));
      }
    }
    STAMP(wave == 0 ? 2 + 12 * layer : 31);
    REFRESH();
    const int kv0 = kvb;
    const int vk = k_off_lin(lane), vkr = k_off_rows(lane), vv = lane * 16;
    const int qal = L_Q + (lane & 8) * 32 + (lane >> 4) * 64 + (lane & 3) * 16, qar = L_Q + (lane & 8) * 32 + (lane >> 4) * 16;
    int x0, x1, x2, x3, nx = 0;
    {
      const int r0 = (T_Q + (layer == 0 ? 3 * NROW3 : 0) + wave) * 4096, r1 = r0 + 4 * 4096;
      const bool str = LEVEL_STR && layer == 0 && wave != 0;
      const bool sgive = (STRADDLE >> (cu >> 3)) & 1, stake = (STRADDLE >> ((cu >> 3) ^ 1)) & 1;
      x0 = x1 = x2 = x3 = 0;
      auto push = [&](int so) { x3 = x2; x2 = x1; x1 = x0; x0 = so; ++nx; };
      push(kv0 + r0);
      if (!(str && sgive)) push(kv0 + r1);
      if (str && stake) push(((cu ^ 8) << 20) + r1);
    }
    const int nstream = nx - 2 + (layer == 0 ? (wave == 0 ? NROW4 : NROW4 + NROW3) : NQ / 4);
    auto next_off = [&](int n) -> int {
      if (n < nx) return n == 0 ? x0 : n == 1 ? x1 : n == 2 ? x2 : x3;
      const int i = n - nx;
      if (layer == 0) return kv0 + 4096 * (T_Q + (wave == 0 ? 3 * NROW3 + 8 + 4 * i : (i < NROW3 ? 3 * i + wave - 1 : 4 * i - NROW3 + 8 + wave)));
      const int k = i - NPRE, t = (k * L2MUL) >> 8, r = k - L2PER * t;
      const int row = k < 0 ? i + 2 : (k >= L2PER * (NL2 / 4) ? i + 2 - NL2 / 4 : (r == L2PER - 1 ? NQ / 4 - NL2 / 4 + t : i + 2 - t));
      return kv0 + 4096 * (T_Q + wave + 4 * row);
    };
#define ISSUE_Q(S, ST, SEQ) do { const int so_ = next_off(s.next), id_ = (so_ >> 12) & 255; \
      const bool lin_ = layer == 0 ? s.next < LIN_HEAD : (LIN_L2 && (unsigned)(s.next - nx - NPRE) < (unsigned)(L2PER * (NL2 / 4))); \
      if (layer == 0 && (id_ >= 256 - NL2 || ((MALLROWS >> (id_ >> 2)) & 1) != 0)) tile_load<S, P_KEEP, false>(vkr, vv, rk, rv, so_); \
      else if (lin_) tile_load<S, P_NT, true>(vk, vv, rk, rv, so_); \
      else tile_load<S, P_NT, false>(vkr, vv, rk, rv, so_); \
      ST = lin_ ? 5 : 1; \
      ++s.next; s.ops += 8; SEQ = s.ops; } while (0)
    Att a;
    a.k0 = ((lane & 7) >> 1) == 0 ? ((lane & 1) ? 0xffff0000u : 0xffffu) : 0u;
    a.k1 = ((lane & 7) >> 1) == 1 ? ((lane & 1) ? 0xffff0000u : 0xffffu) : 0u;
    a.k2 = ((lane & 7) >> 1) == 2 ? ((lane & 1) ? 0xffff0000u : 0xffffu) : 0u;
    a.k3 = ((lane & 7) >> 1) == 3 ? ((lane & 1) ? 0xffff0000u : 0xffffu) : 0u;
    s.nl = wave < 2 ? 4 : 3;
    s.ldsa = L_TILES + wave * 8192;
    s.next = 0;
#define IMG_DONE() do { unsigned n_ = 0; while (ctl[3] < 6u && spin_more<1>(n_)) {} } while (0)
    if (layer == 0) {
#define PARK(S) do { waitvm<PTHR>(); tile_load<S, P_NT, true>(vk, vv, rk, rv, kv0 + (4 * (S - 5) + wave) * 4096); } while (0)
      if (wave == 0) {
        waitvm<PTHR>(); tile_load<3, P_NT, true>(vk, vv, rk, rv, kv0 + T_W0 * 4096);
        waitvm<PTHR>(); tile_load<4, P_NT, true>(vk, vv, rk, rv, kv0 + (T_W0 + 1) * 4096);
      }
      if (wave == 0) {
        for (int k = 0; k < 3; ++k) { waitvm<PTHR>(); tile_dma(vk, vv, rk, rv, kv0 + (T_LDS + 4 * k) * 4096, L_TILES + 4 * k * 8192); }
        PARK(5); PARK(6); PARK(7); PARK(8); PARK(9); PARK(10); PARK(11); PARK(12); PARK(13);
        IMG_DONE();
        waitvm<PTHR>(); tile_dma(vk, vv, rk, rv, kv0 + (T_LDS + 12) * 4096, L_TILES + 12 * 8192);
        waitvm<PTHR>(); ISSUE_Q(0, s.st0, s.seq0);
        waitvm<PTHR>(); ISSUE_Q(1, s.st1, s.seq1);
        waitvm<PTHR>(); ISSUE_Q(2, s.st2, s.seq2);
      } else {
        const Params* pp = kargs();
        {
          const __amdgpu_buffer_rsrc_t rg = __builtin_amdgcn_make_buffer_rsrc((void*)pp->wg, 0, D * F * 2, 0x00020000);
          const __amdgpu_buffer_rsrc_t ru = __builtin_amdgcn_make_buffer_rsrc((void*)pp->wu, 0, D * F * 2, 0x00020000);
          int vo[3];
#pragma unroll
          for (int k = 0; k < 3; ++k) { const int j = 64 * k + lane, row = (j * 171) >> 9; vo[k] = row * (F * 2) + 16 * (j - 3 * row); }
#define GUGRP(RS, MAT, G) do { _Pragma("unroll") for (int k = 0; k < 3; ++k) \
            dma1k_shared(vo[k], RS, (192 * wp + 64 * G) * (F * 2) + ((36 * mu) & ~15), L_IMG + (MAT * D + 192 * wp + 64 * G) * 48 + 1024 * k); } while (0)
#define PARKD(S) do { waitvm<GUW>(); tile_load<S, P_NT, true>(vk, vv, rk, rv, kv0 + (4 * (S - 5) + wave) * 4096); } while (0)
          ST1(35);
          GUGRP(rg, 0, 0); PARKD(9); GUGRP(rg, 0, 1); PARKD(10); GUGRP(rg, 0, 2); PARKD(11);
          ST1(36);
          GUGRP(ru, 1, 0); PARKD(12); GUGRP(ru, 1, 1); PARKD(13); GUGRP(ru, 1, 2);
#undef PARKD
#undef GUGRP
        }
        ST1(37);
        waitvm<GUW>(); ISSUE_Q(0, s.st0, s.seq0);
        waitvm<GUW - 3>(); ISSUE_Q(1, s.st1, s.seq1);
        waitvm<16>();
        lds_claim(L_CTL + 12);
        unsigned spins = 0;
        while (ctl[3] < 3u && spin_more<1>(spins)) {}
        asm volatile("" ::: "memory");
        ST1(38);
        LAS unsigned* img = (LAS unsigned*)(lds + L_IMG) + ((lane >> 5) * D + 18 * (lane & 31)) * 12 + ((9 * mu) & 3) + 3 * wp;
        gu_repair<0>(img); gu_repair<1>(img); gu_repair<2>(img); gu_repair<3>(img); gu_repair<4>(img);
        gu_repair<5>(img); gu_repair<6>(img); gu_repair<7>(img); gu_repair<8>(img);
        lds_claim(L_CTL + 12);
        waitvm<PTHR>(); ISSUE_Q(2, s.st2, s.seq2);
        if (wave == 1) { IMG_DONE(); waitvm<PTHR>(); tile_dma(vk, vv, rk, rv, kv0 + (T_LDS + 13) * 4096, L_TILES + 13 * 8192); s.ops += 8; }
      }
#undef PARK
    } else {
      s.seq0 = s.seq1 = s.seq2 = s.ops - 64;
      s.st0 = s.st1 = s.st2 = LIN_KEEP ? 5 : 1;
    }
    if (wave != 0) {
      unsigned spins = 0;
      while (ctl[2] != (unsigned)(layer + 1) && spin_more<1>(spins)) {}
      asm volatile("" ::: "memory");
    }
    a.acc = __builtin_bit_cast(f32x4, zero4()); a.m = -INFINITY; a.l = asf(zero_u());
    STAMP(wave == 0 ? 3 + 12 * layer : (wave == 1 && layer == 0 ? 40 : 31));

    int e = wave == 0 ? 0 : 2;
#define WAIT_SLOT(SEQ) do { const int yg_ = s.ops - SEQ; \
      if (yg_ == 16) waitvm<16>(); else if (yg_ >= 29) waitvm<29>(); else if (yg_ >= 21) waitvm<21>(); else if (yg_ >= 13) waitvm<13>(); \
      else if (yg_ >= 8) waitvm<8>(); else waitvm<0>(); } while (0)
#define STEP(S, ST, SEQ) do { if (ST != 0) { \
      if ((ST & 4) == 0) { const QFrag q_ = q_frag<false>(qar); WAIT_SLOT(SEQ); tile_compute<S, false>(a, q_); } \
      bool lin_ = (ST & 4) != 0, lds_ = (LDSALL || S == 1) && s.nl != 0 && e >= LDSLAP; \
      if (lin_) WAIT_SLOT(SEQ); \
      while (lin_ || lds_) { \
        const QFrag q_ = q_frag<true>(qal); \
        if (!lin_) { tile_from_lds<S>(vv + s.ldsa); s.ldsa += 4 * 8192; --s.nl; lds_ = false; } \
        lin_ = false; \
        tile_compute<S, true>(a, q_); \
      } \
      if (s.next < nstream) { ISSUE_Q(S, ST, SEQ); PROG(); } \
      else if ((ST & 2) == 0 && layer == 0) { \
        tile_load<S, P_NT, LIN_KEEP>(LIN_KEEP ? vk : vkr, vv, rk, rv, kv0 + (T_KEEP + 4 * S + wave) * 4096); s.ops += 8; SEQ = s.ops; ST = LIN_KEEP ? 7 : 3; \
        if (wave != 0 && (s.st0 & s.st1 & s.st2 & 2) != 0) { IMG_DONE(); wowd_dma(cu, mu, wp, vv); s.ops += 13; } } \
      else { ST = 0; } \
    } } while (0)
#define PROG() do {} while (0)
    for (;;) {
      STEP(0, s.st0, s.seq0);
      STEP(1, s.st1, s.seq1);
      STEP(2, s.st2, s.seq2);
      for (int r = 0; r < XPL; ++r, ++e) {
        if (e >= 11) continue;
        const QFrag q = q_frag<true>(qal);
        switch (e) {
          case 0: tile_compute<3, true>(a, q); break;
          case 1: tile_compute<4, true>(a, q); break;
          case 2: tile_compute<5, true>(a, q); break;
          case 3: tile_compute<6, true>(a, q); break;
          case 4: tile_compute<7, true>(a, q); break;
          case 5: tile_compute<8, true>(a, q); break;
          case 6: tile_compute<9, true>(a, q); break;
          case 7: tile_compute<10, true>(a, q); break;
          case 8: tile_compute<11, true>(a, q); break;
          case 9: tile_compute<12, true>(a, q); break;
          default: tile_compute<13, true>(a, q); break;
        }
      }
      if (e >= 11 && (s.st0 | s.st1 | s.st2) == 0) break;
    }
#undef IMG_DONE
#undef STEP
#undef WAIT_SLOT
#undef PROG
#undef ISSUE_Q
    STAMP(wave == 0 ? 4 + 12 * layer : 27 + wave);

    {
      REFRESH();
      LAS float* mrg = (LAS float*)(lds + L_MRG) + 260 * wave;
      const float lsum = xrow_sum(a.l);
      const int h = (lane >> 3) & 1;
#pragma unroll
      for (int r = 0; r < 4; ++r) mrg[128 * h + 32 * (lane >> 4) + 8 * r + (lane & 7)] = a.acc[r];
      if ((lane & 55) == 0) { mrg[256 + 2 * h] = a.m; mrg[257 + 2 * h] = lsum; }
    }
    BAR();
    REFRESH();
    {
      LAS float* mrg = (LAS float*)(lds + L_MRG);
      const int tid = 64 * wave + lane, h = wave >> 1;
      float mw[4], M = -INFINITY;
#pragma unroll
      for (int w = 0; w < 4; ++w) { mw[w] = mrg[260 * w + 256 + 2 * h]; M = fmaxf(M, mw[w]); }
      float o = 0.f, L = 0.f;
#pragma unroll
      for (int w = 0; w < 4; ++w) { const float ew = __builtin_amdgcn_exp2f(mw[w] - M); o = fmaf(ew, mrg[260 * w + tid], o); L = fmaf(ew, mrg[260 * w + 257 + 2 * h], L); }
      __builtin_amdgcn_raw_buffer_store_b64(v2u{asu(o), tg + 1}, rw, rec + R_O + 8 * tid, 0, AUX_SC1);
      if ((tid & 127) == 0) __builtin_amdgcn_raw_buffer_store_b128(v4u{asu(M), tg + 1, asu(L), tg + 1}, rw, rec + R_ML + 16 * h, 0, AUX_SC1);
    }
    STAMP(wave == 0 ? 5 + 12 * layer : 31);
    if (wave == 0) {
      v4u g0 = zero4(), g1 = zero4(), ml = zero4();
      const int off = W_REC + (16 * grp + (lane >> 2)) * REC;
      unsigned spins = 0;
      bool ok = false;
      do {
        if (!ok) {
          g0 = __builtin_amdgcn_raw_buffer_load_b128(rw, off + R_O + 128 * sp + 32 * (lane & 3), 0, AUX_SC1);
          g1 = __builtin_amdgcn_raw_buffer_load_b128(rw, off + R_O + 128 * sp + 32 * (lane & 3) + 16, 0, AUX_SC1);
          ml = __builtin_amdgcn_raw_buffer_load_b128(rw, off + R_ML + 16 * (sp >> 3), 0, AUX_SC1);
          ok = (g0[1] == tg + 1) & (g0[3] == tg + 1) & (g1[1] == tg + 1) & (g1[3] == tg + 1) & (ml[1] == tg + 1) & (ml[3] == tg + 1);
        }
      } while (!__all(ok) && spin_more<SL0>(spins));
      float M = asf(ml[0]);
      M = fmaxf(M, dpp<ROR4>(M)); M = fmaxf(M, dpp<ROR8>(M)); M = xrow_max(M);
      const float w = __builtin_amdgcn_exp2f(asf(ml[0]) - M);
      float v[5] = {w * asf(g0[0]), w * asf(g0[2]), w * asf(g1[0]), w * asf(g1[2]), w * asf(ml[2])};
#pragma unroll
      for (int k = 0; k < 5; ++k) { float t = v[k]; t += dpp<ROR4>(t); t += dpp<ROR8>(t); v[k] = xrow_sum(t); }
      const float il = __builtin_amdgcn_rcpf(v[4]);
      if (lane < 4) *(LAS v2u*)(lds + L_O + 8 * lane) = v2u{f2bf(v[0] * il) | (f2bf(v[1] * il) << 16), f2bf(v[2] * il) | (f2bf(v[3] * il) << 16)};
    } else {
      waitvm<0>();
    }
    BAR();
    REFRESH();
    STAMP(wave == 0 ? 6 + 12 * layer : 31);
    {
      const u64 t0 = __builtin_amdgcn_s_memrealtime();
      if (wave != 0) rows_gemv_publish<2, 8>(lds, L_WO, L_O, wp, lane, rw, rec + R_Y, tg + 2);
      if (isred) {
        pause_until(t0, DLY_Y1);
        gather_y<SL_Y>(lds, rw, rho, wave, lane, tg + 2);
        BAR();
        if (wave == 0) __builtin_amdgcn_raw_buffer_store_b64(v2u{finish_x(lds, lane), tg + 3}, rw, W_X + (lane >> 3) * XREP + 64 * rho + 8 * (lane & 7), 0, AUX_SC1);
        STAMP(wave == 0 ? 7 + 12 * layer : 31);
      }
      if (wave == 0) {
        REFRESH();
        v2u xp[3];
        pause_until(t0, DLY_X1);
        gather_x<SL_X>(rw, cu, lane, tg + 3, xp);
        STAMP(8 + 12 * layer);
        layernorm_to_lds(xp, lds, lane);
      }
    }
    BAR();
    REFRESH();
    STAMP(wave == 0 ? 9 + 12 * layer : 31);
    if (wave != 0) {
      float acc[6];
#pragma unroll
      for (int k = 0; k < 6; ++k) acc[k] = asf(zero_u());
      LAS unsigned* ln2 = (LAS unsigned*)(lds + L_LN) + 9 * (lane & 31);
      unsigned in2[9];
#pragma unroll
      for (int i = 0; i < 9; ++i) in2[i] = ln2[i];
      gu_pair<0>(acc, in2[0]); gu_pair<1>(acc, in2[1]); gu_pair<2>(acc, in2[2]); gu_pair<3>(acc, in2[3]); gu_pair<4>(acc, in2[4]);
      gu_pair<5>(acc, in2[5]); gu_pair<6>(acc, in2[6]); gu_pair<7>(acc, in2[7]); gu_pair<8>(acc, in2[8]);
      asm volatile("s_nop 2" : "+v"(acc[0]), "+v"(acc[1]), "+v"(acc[2]), "+v"(acc[3]), "+v"(acc[4]), "+v"(acc[5]));
      unsigned hb[3];
#pragma unroll
      for (int k = 0; k < 3; ++k) {
        float t = swap16_add(acc[k], acc[k + 3]);
        t += dpp<XOR1>(t); t += dpp<XOR2>(t); t += dpp<ROR4>(t); t += dpp<ROR8>(t);
        auto sw = __builtin_amdgcn_permlane32_swap(asu(t), asu(t), false, false);
        const float g = rbf(asf(sw[0])), u = rbf(asf(sw[1]));
        hb[k] = f2bf((g > 0.f ? g : g * 0.f) * u);
      }
      if ((lane & 47) == 0) {
        LAS u16* h = (LAS u16*)(lds + L_H) + 6 * wp + 3 * (lane >> 4);
        h[0] = (u16)hb[0]; h[1] = (u16)hb[1]; h[2] = (u16)hb[2];
      }
    }
    BAR();
    REFRESH();
    STAMP(wave == 0 ? 10 + 12 * layer : 31);
    {
      const u64 t0 = __builtin_amdgcn_s_memrealtime();
      if (wave != 0) rows_gemv_publish<3, 9>(lds, L_WD, L_H, wp, lane, rw, rec + R_Y, tg + 4);
      if (isred) {
        pause_until(t0, DLY_Y2);
        gather_y<SL_Y>(lds, rw, rho, wave, lane, tg + 4);
        BAR();
        if (wave == 0) {
          const unsigned xn = finish_x(lds, lane);
          if (layer == 0) {
            __builtin_amdgcn_raw_buffer_store_b64(v2u{xn, 5u}, rw, W_X + (lane >> 3) * XREP + 64 * rho + 8 * (lane & 7), 0, AUX_SC1);
          } else {
            const __amdgpu_buffer_rsrc_t ro = __builtin_amdgcn_make_buffer_rsrc((void*)kargs()->out, 0, D * 2, 0x00020000);
            if (lane < 8) __builtin_amdgcn_raw_buffer_store_b32(xn, ro, 2 * piece_col(4 * rho + (lane >> 1)) + 4 * (lane & 1), 0, 0);
            if (cu == 0 && lane == 0) { const u64 en = (((u64)ctl[5] << 32) | ctl[4]) + 2; __builtin_amdgcn_raw_buffer_store_b64(v2u{(unsigned)en, (unsigned)(en >> 32)}, rw, W_EPOCH, 0, AUX_SC1); }
          }
        }
        STAMP(wave == 0 ? 11 + 12 * layer : 31);
      }
      if (wave == 0 && layer == 0) {
        REFRESH();
        v2u xp[3];
        pause_until(t0, DLY_X2);
        gather_x<SL_X>(rw, cu, lane, 5, xp);
        STAMP(12);
        layernorm_to_lds(xp, lds, lane);
      }
    }
  }
  asm volatile("" :: "a"(pin0), "a"(pin1));
}

#undef REFRESH
#undef grp
#undef sp
#undef wp
#undef mu
#undef kvb
#undef rec
#undef rho
#undef isred

}

extern "C" void kernel_launch(void* const* d_in, const int* in_sizes, int n_in, void* d_out, int out_size, void* d_ws, size_t ws_size,
                              hipStream_t stream) {
  Params p;
  p.x = (const u16*)d_in[0]; p.K = (const u16*)d_in[1]; p.V = (const u16*)d_in[2]; p.wq = (const u16*)d_in[3]; p.wo = (const u16*)d_in[4];
  p.wg = (const u16*)d_in[6]; p.wu = (const u16*)d_in[7]; p.wd = (const u16*)d_in[8];
  p.out = (u16*)d_out; p.ws = (unsigned char*)d_ws;
  (void)hipFuncSetAttribute((const void*)fused_decode, hipFuncAttributeMaxDynamicSharedMemorySize, LDS_BYTES);
  hipLaunchKernelGGL(fused_decode, dim3(NCU), dim3(256), LDS_BYTES, stream, p);
}
```
